```python
import torch

def _softmax(x, axis=-1):
    return torch.softmax(x, dim=axis)
def _gelu(x):
    return torch.nn.functional.gelu(x, approximate="tanh")
def _silu(x):
    return torch.nn.functional.silu(x)
def _sigmoid(x):
    return torch.sigmoid(x)
def _layernorm(x, axis=-1, eps=1e-5):
    m = x.mean(dim=axis, keepdim=True)
    v = x.var(dim=axis, keepdim=True, unbiased=False)
    return (x - m) / torch.sqrt(v + eps)
def _scatter_add(tgt, idx, src, axis=0):
    return tgt.clone().index_add_(axis, idx, src)
def _segment_sum(src, seg, n):
    out = torch.zeros((n,) + src.shape[1:], dtype=src.dtype)
    return out.index_add_(0, seg, src)
def _pad(x, pw):
    flat = tuple(v for pair in reversed(pw) for v in pair)
    return torch.nn.functional.pad(x, flat)

kernel_name = 'parallel_sum__mha_block__glu_gated_geglu__prompt'

def setup_inputs():

    _w = (lambda a: (lambda t: t.to(torch.bfloat16) if t.is_floating_point() else t)(torch.as_tensor(a)))
    def _A_setup():
        B, S, D, H = 1, 2048, 448, 16
        return {
            "x": _w((lambda *s: torch.randn(*s, dtype=torch.bfloat16) * 0.05)(B, S, D)),
            "wqkv": _w((lambda *s: torch.randn(*s, dtype=torch.bfloat16) * 0.05)(D, 3 * D)),
            "wo": _w((lambda *s: torch.randn(*s, dtype=torch.bfloat16) * 0.05)(D, D)),
        }
    _A_in = _A_setup()

    def _B_setup():
        B, S, Dm, Df = 1, 2048, 448, 1792
        return {
            "x": _w((lambda *s: torch.randn(*s, dtype=torch.bfloat16) * 0.05)(B, S, Dm)),
            "w_gate": _w((lambda *s: torch.randn(*s, dtype=torch.bfloat16) * 0.4)(Dm, Df) * (Dm ** -0.5)),
            "w_up": _w((lambda *s: torch.randn(*s, dtype=torch.bfloat16) * 0.4)(Dm, Df) * (Dm ** -0.5)),
            "w_down": _w((lambda *s: torch.randn(*s, dtype=torch.bfloat16) * 0.4)(Df, Dm) * (Df ** -0.5)),
        }
    _B_in = _B_setup()

    return {
        "A_x": _A_in["x"],
        "A_wqkv": _A_in["wqkv"],
        "A_wo": _A_in["wo"],
        "B_x": _B_in["x"],
        "B_w_gate": _B_in["w_gate"],
        "B_w_up": _B_in["w_up"],
        "B_w_down": _B_in["w_down"],
    }

def reference(A_x, A_wqkv, A_wo, B_x, B_w_gate, B_w_up, B_w_down):
    def _A_ref(x, wqkv, wo):
        B, S, D = x.shape
        H = 16
        Dh = D // H
        qkv = (lambda x, *d: x.permute(*d))((x @ wqkv).reshape(B, S, 3, H, Dh), 2, 0, 3, 1, 4)
        q, k, v = qkv[0], qkv[1], qkv[2]
        scores = torch.einsum("bhqd,bhkd->bhqk", q, k) * (Dh ** -0.5)
        p = _softmax(scores, axis=-1)
        o = torch.einsum("bhqk,bhkd->bhqd", p, v)
        return (lambda x, *d: x.permute(*d))(o, 0, 2, 1, 3).reshape(B, S, D) @ wo

    def _B_ref(x, w_gate, w_up, w_down):
        g = torch.einsum("bsd,df->bsf", x, w_gate)
        u = torch.einsum("bsd,df->bsf", x, w_up)
        h = _gelu(g) * u
        return torch.einsum("bsf,fd->bsd", h, w_down)

    x = A_x
    xn = _layernorm(x, axis=-1)
    return x + _A_ref(xn, A_wqkv, A_wo) + _B_ref(xn, B_w_gate, B_w_up, B_w_down)
```

```cpp
#include <hip/hip_runtime.h>
#include <hip/hip_bf16.h>
#include <stdint.h>
typedef __hip_bfloat16 bf16;
constexpr int S=2048, D=448, H=16, DH=28, DF=1792;
static __device__ inline float b2f(bf16 x){return __bfloat162float(x);}
__global__ void k_ln(const bf16* x, bf16* xn){
  int r=blockIdx.x; int t=threadIdx.x;
  float v[7]; float s=0;
  for(int i=0;i<7;i++){v[i]=b2f(x[r*D+t+64*i]); s+=v[i];}
  for(int o=32;o;o>>=1) s+=__shfl_xor(s,o);
  float m=s/D; float q=0;
  for(int i=0;i<7;i++){float d=v[i]-m; q+=d*d;}
  for(int o=32;o;o>>=1) q+=__shfl_xor(q,o);
  float rs=rsqrtf(q/D+1e-5f);
  for(int i=0;i<7;i++) xn[r*D+t+64*i]=__float2bfloat16((v[i]-m)*rs);
}
__global__ void k_gemm(const bf16* A, const bf16* B, float* C, int M, int N, int K){
  int n=blockIdx.x*64+threadIdx.x; int m=blockIdx.y;
  if(n>=N) return;
  float acc=0;
  for(int k=0;k<K;k++) acc+=b2f(A[m*K+k])*b2f(B[k*N+n]);
  C[m*N+n]=acc;
}
__global__ void k_attn(const float* qkv, bf16* o){
  int q=blockIdx.x, h=blockIdx.y, t=threadIdx.x;
  float qv[DH];
  for(int d=0;d<DH;d++) qv[d]=b2f(__float2bfloat16(qkv[q*3*D+h*DH+d]));
  float sc[32]; float mx=-1e30f;
  for(int i=0;i<32;i++){int k=t+64*i; float s=0; for(int d=0;d<DH;d++) s+=qv[d]*b2f(__float2bfloat16(qkv[k*3*D+D+h*DH+d])); s*=0.1889822365046136f; sc[i]=s; mx=fmaxf(mx,s);}
  for(int of=32;of;of>>=1) mx=fmaxf(mx,__shfl_xor(mx,of));
  float l=0; float acc[DH]; for(int d=0;d<DH;d++) acc[d]=0;
  for(int i=0;i<32;i++){int k=t+64*i; float p=__expf(sc[i]-mx); l+=p; for(int d=0;d<DH;d++) acc[d]+=p*b2f(__float2bfloat16(qkv[k*3*D+2*D+h*DH+d]));}
  for(int of=32;of;of>>=1) l+=__shfl_xor(l,of);
  for(int d=0;d<DH;d++){float a=acc[d]; for(int of=32;of;of>>=1) a+=__shfl_xor(a,of); if(t==0) o[q*D+h*DH+d]=__float2bfloat16(a/l);}
}
__global__ void k_geglu(const float* g, const float* u, bf16* h, int n){
  int i=blockIdx.x*256+threadIdx.x; if(i>=n) return;
  float x=g[i]; float t=tanhf(0.7978845608f*(x+0.044715f*x*x*x)); h[i]=__float2bfloat16(0.5f*x*(1+t)*u[i]);
}
__global__ void k_final(const bf16* x, const float* a, const float* f, bf16* out, int n){
  int i=blockIdx.x*256+threadIdx.x; if(i>=n) return;
  out[i]=__float2bfloat16(b2f(x[i])+a[i]+f[i]);
}
extern "C" void kernel_launch(void* const* d_in, const int* in_sizes, int n_in, void* d_out, int out_size, void* d_ws, size_t ws_size, hipStream_t stream) {
  const bf16* x=(const bf16*)d_in[0]; const bf16* wqkv=(const bf16*)d_in[1]; const bf16* wo=(const bf16*)d_in[2];
  const bf16* wg=(const bf16*)d_in[4]; const bf16* wu=(const bf16*)d_in[5]; const bf16* wd=(const bf16*)d_in[6];
  char* ws=(char*)d_ws;
  bf16* xn=(bf16*)ws; ws+=S*D*2;
  float* qkv=(float*)ws; ws+=S*3*D*4;
  float* g=(float*)ws; ws+=S*DF*4;
  float* u=(float*)ws; ws+=S*DF*4;
  bf16* hh=(bf16*)ws; ws+=S*DF*2;
  bf16* o=(bf16*)ws; ws+=S*D*2;
  float* a=(float*)ws; ws+=S*D*4;
  float* f=(float*)ws; ws+=S*D*4;
  k_ln<<<S,64,0,stream>>>(x,xn);
  k_gemm<<<dim3(21,S),64,0,stream>>>(xn,wqkv,qkv,S,3*D,D);
  k_gemm<<<dim3(28,S),64,0,stream>>>(xn,wg,g,S,DF,D);
  k_gemm<<<dim3(28,S),64,0,stream>>>(xn,wu,u,S,DF,D);
  k_geglu<<<S*DF/256,256,0,stream>>>(g,u,hh,S*DF);
  k_attn<<<dim3(S,H),64,0,stream>>>(qkv,o);
  k_gemm<<<dim3(7,S),64,0,stream>>>(o,wo,a,S,D,D);
  k_gemm<<<dim3(7,S),64,0,stream>>>(hh,wd,f,S,D,DF);
  k_final<<<S*D/256,256,0,stream>>>(x,a,f,(bf16*)d_out,S*D);
}
```

```cpp
#include <hip/hip_runtime.h>
#include <hip/hip_bf16.h>
#include <stdint.h>

typedef __attribute__((ext_vector_type(8))) __bf16 bf16x8;
typedef __attribute__((ext_vector_type(4))) __bf16 bf16x4;
typedef __attribute__((ext_vector_type(2))) __bf16 bf16x2;
typedef __attribute__((ext_vector_type(4))) short s16x4;
typedef __attribute__((ext_vector_type(8))) short s16x8;
typedef __attribute__((ext_vector_type(2))) float f32x2;
typedef __attribute__((ext_vector_type(4))) float f32x4;
typedef __attribute__((ext_vector_type(8))) float f32x8;
typedef __attribute__((ext_vector_type(16))) float f32x16;
typedef __attribute__((ext_vector_type(2))) unsigned u32x2;
typedef __attribute__((ext_vector_type(4))) unsigned u32x4;
typedef __attribute__((address_space(3))) char* ldsp;
typedef unsigned short u16;
#define DI __device__ __forceinline__

constexpr int S = 2048, D = 448, DF = 1792, NH = 16, DH = 28;
constexpr size_t WS_Q   = 0;
constexpr size_t WS_KV  = WS_Q + (2u << 20);
constexpr size_t WS_H   = WS_KV + (4u << 20);
constexpr size_t WS_O   = WS_H + 64 * 112 * 1024;
constexpr size_t WS_WD  = WS_O + (2u << 20);
constexpr size_t WS_WO  = WS_WD + 112 * 14 * 1024;
constexpr size_t WS_FF  = WS_WO + 32 * 14 * 1024;
constexpr size_t WS_ST  = WS_FF + 64 * 14 * 4096;
constexpr size_t WS_CS  = WS_ST + 2048 * 8;
constexpr size_t WS_END = WS_CS + 16 * 320 * 4;

DI float bf2f(u16 v) { return __uint_as_float((unsigned)v << 16); }
DI unsigned pack2(float a, float b) {
  f32x2 v = {a, b};
  return __builtin_bit_cast(unsigned, __builtin_convertvector(v, bf16x2));
}
DI int rfl(int v) { return __builtin_amdgcn_readfirstlane(v); }
DI __amdgpu_buffer_rsrc_t mkrsrc(const void* p, unsigned bytes) {
  return __builtin_amdgcn_make_buffer_rsrc((void*)p, (short)0, (int)bytes, 0x00020000);
}
DI void glds16(__amdgpu_buffer_rsrc_t rs, unsigned voff, unsigned soff, unsigned ldsaddr) {
  asm volatile("s_mov_b32 m0, %0\n\ts_nop 0\n\tbuffer_load_dwordx4 %1, %2, %3 offen lds"
               :: "s"(ldsaddr), "v"(voff), "s"(rs), "s"(soff) : "m0", "memory");
}
DI s16x4 trread(ldsp p) {
  return __builtin_amdgcn_ds_read_tr16_b64_v4i16((__attribute__((address_space(3))) s16x4*)p);
}
DI bf16x8 cat(s16x4 a, s16x4 b) {
  s16x8 r = __builtin_shufflevector(a, b, 0, 1, 2, 3, 4, 5, 6, 7);
  return __builtin_bit_cast(bf16x8, r);
}
#define MFMA16(a, b, c) __builtin_amdgcn_mfma_f32_16x16x32_bf16((a), (b), (c), 0, 0, 0)
#define MFMA32(a, b, c) __builtin_amdgcn_mfma_f32_32x32x16_bf16((a), (b), (c), 0, 0, 0)

DI int hfeat(int ks16, int h, int j) {
  int ks32 = ks16 >> 1, g = (ks16 & 1) * 2 + h;
  if (ks32 < 48) { int cb = ks32 / 3, n = ks32 % 3; return cb * 112 + (2 * n + (j >> 2)) * 16 + 4 * g + (j & 3); }
  return (2 * (ks32 - 48) + (j >> 2)) * 112 + 96 + 4 * g + (j & 3);
}

__global__ __launch_bounds__(256) void k0(const u16* x, const u16* wqkv, const u16* wo, const u16* wg, const u16* wu,
                                          const u16* wd, char* ws) {
  int b = blockIdx.x, t = threadIdx.x, l = t & 63, w = t >> 6;
  if (b < 512) {
    int r = b * 4 + w;
    float v[7], s = 0;
    for (int i = 0; i < 7; i++) { v[i] = bf2f(x[r * D + l + 64 * i]); s += v[i]; }
    for (int o = 32; o; o >>= 1) s += __shfl_xor(s, o);
    float m = s * (1.0f / D), q = 0;
    for (int i = 0; i < 7; i++) { float d = v[i] - m; q += d * d; }
    for (int o = 32; o; o >>= 1) q += __shfl_xor(q, o);
    if (l == 0) { f32x2 o2 = {m, q * (1.0f / D) + 1e-5f}; ((f32x2*)(ws + WS_ST))[r] = o2; }
  } else if (b < 512 + 80) {
    __shared__ float red[4][64];
    int c = (b - 512) * 64 + l;
    int cb = c / 320, i = c % 320;
    const u16* p; int stride;
    if (i < 112) { p = wg + cb * 112 + i; stride = DF; }
    else if (i < 224) { p = wu + cb * 112 + (i - 112); stride = DF; }
    else { int pp = (i - 224) >> 5, d = (i - 224) & 31; if (d >= DH) d = 0; p = wqkv + pp * D + cb * DH + d; stride = 3 * D; }
    float s = 0;
    for (int k = w * 112; k < w * 112 + 112; k++) s += bf2f(p[k * stride]);
    red[w][l] = s; __syncthreads();
    if (w == 0) ((float*)(ws + WS_CS))[c] = red[0][l] + red[1][l] + red[2][l] + red[3][l];
  } else {
    int f = (b - 592) * 4 + w;
    s16x8 v;
    if (f < 112 * 14) {
      int ks = f / 14, nt = f % 14;
      for (int j = 0; j < 8; j++) v[j] = wd[hfeat(ks, l >> 5, j) * D + nt * 32 + (l & 31)];
      *(s16x8*)(ws + WS_WD + f * 1024 + l * 16) = v;
    } else {
      f -= 112 * 14;
      int ks = f / 14, nt = f % 14, head = ks >> 1, s = ks & 1;
      for (int j = 0; j < 8; j++) {
        int d = 16 * s + 8 * (j >> 2) + 4 * (l >> 5) + (j & 3);
        v[j] = d < DH ? wo[(head * DH + d) * D + nt * 32 + (l & 31)] : 0;
      }
      *(s16x8*)(ws + WS_WO + f * 1024 + l * 16) = v;
    }
  }
}

constexpr int WSLOT = 20480, NWS = 5, XRING = NWS * WSLOT, XSLOT = 16384, VIMG = XRING + 3 * XSLOT, K1_LDS = VIMG + 8192;
constexpr int k1_nw(int role) { return role == 0 ? 3 : 2; }
constexpr int k1_allow(int s, int role) {
  int nw = k1_nw(role);
  int pos = 0, needW = 0, needX = 0;
  int st = s + 1, xs = (s + 1) >> 1;
  pos += 2; if (xs == 0) needX = pos;
  pos += nw; if (st == 0) needW = pos;
  pos += nw; if (st == 1) needW = pos;
  pos += 2; if (xs == 1) needX = pos;
  pos += nw; if (st == 2) needW = pos;
  pos += nw; if (st == 3) needW = pos;
  pos += 2; if (xs == 2) needX = pos;
  pos += nw; if (st == 4) needW = pos;
  for (int t = 0; t < s; t++) {
    if (t >= 1) { int X = (t - 1) / 2 + 3; if (X < 7) { pos += 1; if (X == xs) needX = pos; } }
    if (t + 5 < 14) { pos += nw; if (t + 5 == st) needW = pos; }
  }
  int need = needW > needX ? needW : needX;
  return pos - need;
}
constexpr int k1_wait(int s) { int a = k1_allow(s, 0), b = k1_allow(s, 1); return a < b ? a : b; }

template <int N> DI void wait_vm_bar() { asm volatile("s_waitcnt vmcnt(%0) lgkmcnt(0)\n\ts_barrier" :: "n"(N) : "memory"); }

__global__ __launch_bounds__(512, 2) void k1(const u16* x, const u16* wqkv, const u16* wg, const u16* wu, char* ws) {
  extern __shared__ __attribute__((aligned(16))) char smem[];
  const ldsp lds = (ldsp)smem;
  const unsigned lb = (unsigned)(size_t)lds;
  const int b = blockIdx.x, tid = threadIdx.x, l = tid & 63, w = rfl(tid >> 6);
  const int xcd = b & 7, idx = b >> 3;
  const int rb = (xcd >> 2) * 8 + (idx >> 2), cb = (xcd & 3) * 4 + (idx & 3);
  const int mh = w >> 2, n = w & 3, g = l >> 4, l15 = l & 15;

  f32x2 st[4]; f32x4 cs[5];
  {
    const f32x2* sp = (const f32x2*)(ws + WS_ST) + rb * 128 + mh * 64 + l15;
    for (int m = 0; m < 4; m++) st[m] = sp[16 * m];
    const float* cp = (const float*)(ws + WS_CS) + cb * 320 + 4 * g;
    int t1 = n < 3 ? 2 * n : 18, t2 = n < 3 ? 7 + 2 * n : 19, t3 = n < 3 ? 2 * n + 1 : 6;
    cs[0] = *(const f32x4*)(cp + (14 + n) * 16);
    cs[1] = *(const f32x4*)(cp + t1 * 16);
    cs[2] = *(const f32x4*)(cp + t2 * 16);
    cs[3] = *(const f32x4*)(cp + t3 * 16);
    cs[4] = *(const f32x4*)(cp + (t3 + 7) * 16);
  }

  const __amdgpu_buffer_rsrc_t rsX = mkrsrc(x, S * D * 2);
  const unsigned voffX = (rb * 128 + 8 * w + (l >> 3)) * 896 + (((l & 7) ^ ((4 * w + (l >> 4)) & 7)) << 4);
  __amdgpu_buffer_rsrc_t rsW[3]; unsigned voffW[3], strW[3];
#pragma unroll
  for (int j = 0; j < 3; j++) {
    int i = w + 8 * j;
    if (i < 14) {
      int m = i < 7 ? i : i - 7, nn = m * 64 + l, rho = nn / 14, ch = nn - rho * 14;
      int k = (rho & 0x13) | ((rho & 4) << 1) | ((rho & 8) >> 1);
      voffW[j] = k * 3584 + cb * 224 + ch * 16; strW[j] = 32 * 3584;
      rsW[j] = mkrsrc(i < 7 ? wg : wu, D * DF * 2);
    } else {
      int p = (i - 14) >> 1, rho = ((i - 14) & 1) * 16 + (l >> 2), c = (l & 3) ^ ((rho >> 1) & 2);
      int k = (rho & 0x13) | ((rho & 4) << 1) | ((rho & 8) >> 1);
      voffW[j] = k * 2688 + p * 896 + cb * 56 + (c < 3 ? c * 16 : 40); strW[j] = 32 * 2688;
      rsW[j] = mkrsrc(wqkv, D * 3 * D * 2);
    }
  }
  auto issueX = [&](int X, int half) { glds16(rsX, voffX, half * 64 * 896 + X * 128, lb + XRING + (X % 3) * XSLOT + half * 8192 + w * 1024); };
  auto issueW = [&](int s) {
    unsigned d = lb + (s % NWS) * WSLOT + w * 1024;
    glds16(rsW[0], voffW[0], s * strW[0], d);
    glds16(rsW[1], voffW[1], s * strW[1], d + 8192);
    if (w < 4) glds16(rsW[2], voffW[2], s * strW[2], d + 16384);
  };
  issueX(0, 0); issueX(0, 1); issueW(0); issueW(1); issueX(1, 0); issueX(1, 1); issueW(2); issueW(3);
  issueX(2, 0); issueX(2, 1); issueW(4);

  const int q = l15 >> 2, p = l & 3, rho0 = ((g >> 1) << 4) | ((g & 1) << 2) | q;
  auto qkvaddr = [&](int t) { int img = t >> 1, c = t & 1; return 14336 + img * 2048 + rho0 * 64 + ((c ^ (g & 1)) << 5) + ((c ? (p ^ (p >> 1)) : p) << 3); };
  const int gu = rho0 * 224 + p * 8;
  const int a0 = qkvaddr(n);
  const int a1 = n < 3 ? gu + 2 * n * 32 : qkvaddr(4), a2 = n < 3 ? gu + 7168 + 2 * n * 32 : qkvaddr(5);
  const int t12 = n < 3 ? 1792 : 512;
  const int a1b = a1 + t12, a2b = a2 + t12;
  const int a3 = gu + (n < 3 ? 2 * n + 1 : 6) * 32;
  const int ax = XRING + (mh * 64 + l15) * 128 + ((g ^ (l15 >> 1)) << 4);

  f32x4 acc[5][4];
#pragma unroll
  for (int t = 0; t < 5; t++)
#pragma unroll
    for (int m = 0; m < 4; m++) acc[t][m] = (f32x4){0.f, 0.f, 0.f, 0.f};
  bf16x8 wf[2][5], xf[2][4];
  auto rd = [&](int s) {
    const int o = (s % NWS) * WSLOT, bsel = s & 1;
    const int xo = ((s >> 1) % 3) * XSLOT;
#pragma unroll
    for (int m = 0; m < 4; m++)
      xf[bsel][m] = *(const __attribute__((address_space(3))) bf16x8*)(lds + ((ax ^ (bsel << 6)) + xo + m * 2048));
    wf[bsel][0] = cat(trread(lds + (a0 + o)), trread(lds + (a0 + o + 512)));
    wf[bsel][1] = cat(trread(lds + (a1 + o)), trread(lds + (a1b + o)));
    wf[bsel][2] = cat(trread(lds + (a2 + o)), trread(lds + (a2b + o)));
    wf[bsel][3] = cat(trread(lds + (a3 + o)), trread(lds + (a3 + o + 1792)));
    wf[bsel][4] = cat(trread(lds + (a3 + o + 7168)), trread(lds + (a3 + o + 7168 + 1792)));
  };
  wait_vm_bar<k1_wait(-1)>();
  rd(0);
#define K1_STEP(s)                                                             \
  {                                                                            \
    if (s < 13) wait_vm_bar<k1_wait(s < 13 ? s : 0)>();                        \
    if (s >= 1 && (s - 1) / 2 + 3 < 7) issueX((s - 1) / 2 + 3, (s - 1) & 1);   \
    if (s + 5 < 14) issueW(s + 5);                                             \
    if (s < 13) rd(s + 1);                                                     \
    _Pragma("unroll") for (int t = 0; t < 5; t++)                              \
      _Pragma("unroll") for (int m = 0; m < 4; m++)                            \
        acc[t][m] = MFMA16(wf[s & 1][t], xf[s & 1][m], acc[t][m]);             \
  }
  K1_STEP(0) K1_STEP(1) K1_STEP(2) K1_STEP(3) K1_STEP(4) K1_STEP(5) K1_STEP(6)
  K1_STEP(7) K1_STEP(8) K1_STEP(9) K1_STEP(10) K1_STEP(11) K1_STEP(12) K1_STEP(13)

  float rs[4];
#pragma unroll
  for (int m = 0; m < 4; m++) rs[m] = __builtin_amdgcn_rsqf(st[m][1]);
  const int tt0 = rb * 4 + mh * 2;
  {
    const float sc = n < 2 ? 0.18898223650461362f * 1.4426950408889634f : 1.0f;
    char* dst = ws + (n < 2 ? WS_Q + (size_t)cb * 64 * 2048 : WS_KV + (size_t)cb * 64 * 4096) + (n & 1) * 1024 + (l15 + 32 * (g >> 1)) * 16 + (g & 1) * 8;
    const int tstride = n < 2 ? 2048 : 4096;
    const bool pad = (n & 1) && g == 3;
#pragma unroll
    for (int m = 0; m < 4; m++) {
      float f = rs[m] * sc, v[4];
#pragma unroll
      for (int i = 0; i < 4; i++) v[i] = pad ? 0.f : (acc[0][m][i] - st[m][0] * cs[0][i]) * f;
      u32x2 o = {pack2(v[0], v[1]), pack2(v[2], v[3])};
      *(u32x2*)(dst + (tt0 + (m >> 1)) * tstride + (m & 1) * 256) = o;
    }
  }
  auto geglu = [&](int tg, int tu, int m) {
    const float k1c = -2.0f * 0.7978845608028654f * 1.4426950408889634f * rs[m];
    const float k2c = k1c * 0.044715f * rs[m] * rs[m];
    float hv[4];
#pragma unroll
    for (int i = 0; i < 4; i++) {
      float a = acc[tg][m][i] - st[m][0] * cs[tg][i];
      float u = acc[tu][m][i] - st[m][0] * cs[tu][i];
      float z = a * (k2c * (a * a) + k1c);
      float e = __builtin_amdgcn_exp2f(z);
      float r = __builtin_amdgcn_rcpf(e * st[m][1] + st[m][1]);
      hv[i] = a * u * r;
    }
    u32x2 o = {pack2(hv[0], hv[1]), pack2(hv[2], hv[3])};
    return o;
  };
  char* hdst = ws + WS_H + (g >> 1) * 1024 + (l15 + 32 * (g & 1)) * 16;
  if (n < 3) {
#pragma unroll
    for (int m = 0; m < 4; m++) {
      u32x2 o1 = geglu(1, 2, m), o2 = geglu(3, 4, m);
      u32x4 o = {o1[0], o1[1], o2[0], o2[1]};
      *(u32x4*)(hdst + (size_t)(tt0 + (m >> 1)) * (112 * 1024) + (cb * 3 + n) * 2048 + (m & 1) * 256) = o;
    }
  } else {
#pragma unroll
    for (int m = 0; m < 4; m++) {
#pragma unroll
      for (int c = 0; c < 2; c++) {
        float v[4];
#pragma unroll
        for (int i = 0; i < 4; i++) v[i] = (acc[1 + c][m][i] - st[m][0] * cs[1 + c][i]) * rs[m];
        u32x2 o = {pack2(v[0], v[1]), pack2(v[2], v[3])};
        if (c == 1 && g == 3) { o[0] = 0x3f80u; o[1] = 0; }
        int row = mh * 64 + 16 * m + l15;
        *(__attribute__((address_space(3))) u32x2*)(lds + (VIMG + row * 64 + (((4 * c + g) ^ ((row >> 1) & 7)) << 3))) = o;
      }
    }
#pragma unroll
    for (int m = 0; m < 4; m++) {
      u32x2 o = geglu(3, 4, m);
      *(u32x2*)(hdst + (size_t)(tt0 + (m >> 1)) * (112 * 1024) + (48 + (cb >> 1)) * 2048 + (m & 1) * 256 + (cb & 1) * 8) = o;
    }
  }
  __syncthreads();
  {
    const int kt = w >> 1, s = w & 1, blk = (l >> 4) & 1, h = l >> 5;
    const int r0 = 32 * kt + 16 * s + 4 * h + q, r1 = r0 + 8;
    s16x4 lo = trread(lds + (VIMG + r0 * 64 + (((4 * blk + p) ^ ((r0 >> 1) & 7)) << 3)));
    s16x4 hi = trread(lds + (VIMG + r1 * 64 + (((4 * blk + p) ^ ((r1 >> 1) & 7)) << 3)));
    *(bf16x8*)(ws + WS_KV + (size_t)(cb * 64 + rb * 4 + kt) * 4096 + (2 + s) * 1024 + l * 16) = cat(lo, hi);
  }
}

constexpr int K2_LDS = 2 * 49152;
DI bf16x8 pack8(const f32x16& s, int o) {
  u32x4 r = {pack2(s[o], s[o + 1]), pack2(s[o + 2], s[o + 3]), pack2(s[o + 4], s[o + 5]), pack2(s[o + 6], s[o + 7])};
  return __builtin_bit_cast(bf16x8, r);
}
DI void xwrite(ldsp base, const f32x16 (&a)[4], int w4, int l) {
#pragma unroll
  for (int t = 0; t < 4; t++) {
    if (t != w4) {
      int slot = (w4 - t - 1) & 3;
#pragma unroll
      for (int qd = 0; qd < 4; qd++) {
        f32x4 v = {a[t][4 * qd], a[t][4 * qd + 1], a[t][4 * qd + 2], a[t][4 * qd + 3]};
        *(__attribute__((address_space(3))) f32x4*)(base + ((t * 3 + slot) * 4096 + qd * 1024 + l * 16)) = v;
      }
    }
  }
}
DI f32x16 xread(ldsp base, const f32x16 (&a)[4], int w4, int l) {
  f32x16 r = w4 == 0 ? a[0] : w4 == 1 ? a[1] : w4 == 2 ? a[2] : a[3];
#pragma unroll
  for (int slot = 0; slot < 3; slot++)
#pragma unroll
    for (int qd = 0; qd < 4; qd++) {
      f32x4 v = *(const __attribute__((address_space(3))) f32x4*)(base + ((w4 * 3 + slot) * 4096 + qd * 1024 + l * 16));
#pragma unroll
      for (int i = 0; i < 4; i++) r[4 * qd + i] += v[i];
    }
  return r;
}

__global__ __launch_bounds__(512, 2) void k2(const u16* x, char* ws) {
  extern __shared__ __attribute__((aligned(16))) char smem[];
  const ldsp lds = (ldsp)smem;
  const int b = blockIdx.x, tid = threadIdx.x, l = tid & 63, w = rfl(tid >> 6), w4 = w & 3;
  const int xcd = b & 7, idx = b >> 3;
  f32x16 acc[4];
#pragma unroll
  for (int t = 0; t < 4; t++)
#pragma unroll
    for (int i = 0; i < 16; i++) acc[t][i] = 0.f;
  const int head = xcd * 2 + (idx >> 4), qb = idx & 15;
  const int fr = xcd * 4 + idx / 7, fc = idx % 7;
  const bool ffn_on = idx < 28;
  if (w < 4) {
    const bf16x8* Qp = (const bf16x8*)(ws + WS_Q + (size_t)(head * 64 + qb * 4) * 2048) + l;
    const bf16x8* KVp = (const bf16x8*)(ws + WS_KV + (size_t)(head * 64 + w4 * 16) * 4096) + l;
    bf16x8 Q[4][2];
#pragma unroll
    for (int t = 0; t < 4; t++) { Q[t][0] = Qp[t * 128]; Q[t][1] = Qp[t * 128 + 64]; }
    bf16x8 kv[2][4];
#pragma unroll
    for (int i = 0; i < 4; i++) kv[0][i] = KVp[i * 64];
#pragma unroll 2
    for (int kt = 0; kt < 16; kt++) {
      const int c = kt & 1;
      const int ktn = kt < 15 ? kt + 1 : kt;
#pragma unroll
      for (int i = 0; i < 4; i++) kv[c ^ 1][i] = KVp[ktn * 256 + i * 64];
#pragma unroll
      for (int t = 0; t < 4; t++) {
        f32x16 s;
#pragma unroll
        for (int i = 0; i < 16; i++) s[i] = 0.f;
        s = MFMA32(kv[c][0], Q[t][0], s);
        s = MFMA32(kv[c][1], Q[t][1], s);
#pragma unroll
        for (int i = 0; i < 16; i++) s[i] = __builtin_amdgcn_exp2f(s[i]);
        acc[t] = MFMA32(kv[c][2], pack8(s, 0), acc[t]);
        acc[t] = MFMA32(kv[c][3], pack8(s, 8), acc[t]);
      }
    }
    xwrite(lds, acc, w4, l);
  } else if (ffn_on) {
    const bf16x8* Hp = (const bf16x8*)(ws + WS_H + (size_t)(fr * 2) * 112 * 1024 + w4 * 28 * 1024) + l;
    const bf16x8* Wp = (const bf16x8*)(ws + WS_WD + (size_t)(w4 * 28 * 14 + fc * 2) * 1024) + l;
    constexpr int PD = 7;
    bf16x8 f[PD][4];
#pragma unroll
    for (int s = 0; s < PD; s++) {
      f[s][0] = Hp[s * 64]; f[s][1] = Hp[s * 64 + 112 * 64]; f[s][2] = Wp[s * 14 * 64]; f[s][3] = Wp[s * 14 * 64 + 64];
    }
#pragma unroll
    for (int s = 0; s < 28; s++) {
      const int c = s % PD;
      acc[0] = MFMA32(f[c][2], f[c][0], acc[0]);
      acc[1] = MFMA32(f[c][3], f[c][0], acc[1]);
      acc[2] = MFMA32(f[c][2], f[c][1], acc[2]);
      acc[3] = MFMA32(f[c][3], f[c][1], acc[3]);
      if (s + PD < 28) {
        const int sn = s + PD;
        f[c][0] = Hp[sn * 64]; f[c][1] = Hp[sn * 64 + 112 * 64]; f[c][2] = Wp[sn * 14 * 64]; f[c][3] = Wp[sn * 14 * 64 + 64];
      }
    }
    xwrite(lds + 49152, acc, w4, l);
  }
  __syncthreads();
  if (w < 4) {
    f32x16 o = xread(lds, acc, w4, l);
    float lsum = __shfl(o[12], (l & 31) + 32);
    float r = __builtin_amdgcn_rcpf(lsum);
#pragma unroll
    for (int i = 0; i < 16; i++) o[i] *= r;
    bf16x8* Op = (bf16x8*)(ws + WS_O + (size_t)((qb * 4 + w4) * 32 + head * 2) * 1024) + l;
    Op[0] = pack8(o, 0); Op[64] = pack8(o, 8);
  } else if (ffn_on) {
    f32x16 o = xread(lds + 49152, acc, w4, l);
    const int tt = fr * 2 + (w4 >> 1), nt = fc * 2 + (w4 & 1);
    const u16* xp = x + (tt * 32 + (l & 31)) * D + nt * 32 + 4 * (l >> 5);
    f32x4* Fp = (f32x4*)(ws + WS_FF + (size_t)(tt * 14 + nt) * 4096) + l;
#pragma unroll
    for (int qd = 0; qd < 4; qd++) {
      u32x2 xv = *(const u32x2*)(xp + 8 * qd);
      f32x4 v = {o[4 * qd] + __uint_as_float(xv[0] << 16), o[4 * qd + 1] + __uint_as_float(xv[0] & 0xffff0000u),
                 o[4 * qd + 2] + __uint_as_float(xv[1] << 16), o[4 * qd + 3] + __uint_as_float(xv[1] & 0xffff0000u)};
      Fp[qd * 64] = v;
    }
  }
}

constexpr int K3_LDS = 131072 + 16384;
__global__ __launch_bounds__(512, 2) void k3(char* ws, u16* out) {
  extern __shared__ __attribute__((aligned(16))) char smem[];
  const ldsp lds = (ldsp)smem;
  const unsigned lb = (unsigned)(size_t)lds;
  const int b = blockIdx.x, tid = threadIdx.x, l = tid & 63, w = rfl(tid >> 6);
  const int xcd = b & 7, idx = b >> 3;
  const int fr = xcd * 4 + idx / 7, fc = idx % 7;
  const int tile = w & 3, kh = w >> 2;
  const int tt = fr * 2 + (tile >> 1), nt = fc * 2 + (tile & 1);
  const __amdgpu_buffer_rsrc_t rs = mkrsrc(ws, (unsigned)WS_END);
  const f32x4* Fp = (const f32x4*)(ws + WS_FF + (size_t)(tt * 14 + nt) * 4096) + l;
  f32x4 ff0 = Fp[(2 * kh) * 64], ff1 = Fp[(2 * kh + 1) * 64];
#pragma unroll
  for (int c = 0; c < 4; c++) {
    const int ks = kh * 16 + c * 4 + tile;
    glds16(rs, l * 16, WS_O + (fr * 2 * 32 + ks) * 1024, lb + ks * 4096);
    glds16(rs, l * 16, WS_O + ((fr * 2 + 1) * 32 + ks) * 1024, lb + ks * 4096 + 1024);
    glds16(rs, l * 16, WS_WO + (ks * 14 + fc * 2) * 1024, lb + ks * 4096 + 2048);
    glds16(rs, l * 16, WS_WO + (ks * 14 + fc * 2 + 1) * 1024, lb + ks * 4096 + 3072);
  }
  f32x16 acc;
#pragma unroll
  for (int i = 0; i < 16; i++) acc[i] = 0.f;
  const int ao = kh * 65536 + (tile >> 1) * 1024 + l * 16, aw = kh * 65536 + 2048 + (tile & 1) * 1024 + l * 16;
#define K3_CHUNK(c)                                                                                     \
  {                                                                                                     \
    wait_vm_bar<12 - 4 * c>();                                                                          \
    _Pragma("unroll") for (int s = 0; s < 4; s++) {                                                     \
      bf16x8 of = *(const __attribute__((address_space(3))) bf16x8*)(lds + (ao + (c * 4 + s) * 4096));  \
      bf16x8 wf = *(const __attribute__((address_space(3))) bf16x8*)(lds + (aw + (c * 4 + s) * 4096));  \
      acc = MFMA32(wf, of, acc);                                                                        \
    }                                                                                                   \
  }
  K3_CHUNK(0) K3_CHUNK(1) K3_CHUNK(2) K3_CHUNK(3)
  {
    f32x4 s0, s1;
#pragma unroll
    for (int i = 0; i < 4; i++) { s0[i] = kh ? acc[i] : acc[8 + i]; s1[i] = kh ? acc[4 + i] : acc[12 + i]; }
    *(__attribute__((address_space(3))) f32x4*)(lds + (131072 + w * 2048 + l * 16)) = s0;
    *(__attribute__((address_space(3))) f32x4*)(lds + (131072 + w * 2048 + 1024 + l * 16)) = s1;
    __syncthreads();
    f32x4 r0 = *(const __attribute__((address_space(3))) f32x4*)(lds + (131072 + (w ^ 4) * 2048 + l * 16));
    f32x4 r1 = *(const __attribute__((address_space(3))) f32x4*)(lds + (131072 + (w ^ 4) * 2048 + 1024 + l * 16));
#pragma unroll
    for (int i = 0; i < 4; i++) { r0[i] += (kh ? acc[8 + i] : acc[i]) + ff0[i]; r1[i] += (kh ? acc[12 + i] : acc[4 + i]) + ff1[i]; }
    u16* op = out + (tt * 32 + (l & 31)) * D + nt * 32 + 16 * kh + 4 * (l >> 5);
    u32x2 o0 = {pack2(r0[0], r0[1]), pack2(r0[2], r0[3])}, o1 = {pack2(r1[0], r1[1]), pack2(r1[2], r1[3])};
    *(u32x2*)op = o0; *(u32x2*)(op + 8) = o1;
  }
}

extern "C" void kernel_launch(void* const* d_in, const int* in_sizes, int n_in, void* d_out, int out_size, void* d_ws,
                              size_t ws_size, hipStream_t stream) {
  const u16* x = (const u16*)d_in[0];
  const u16* wqkv = (const u16*)d_in[1];
  const u16* wo = (const u16*)d_in[2];
  const u16* wg = (const u16*)d_in[4];
  const u16* wu = (const u16*)d_in[5];
  const u16* wd = (const u16*)d_in[6];
  char* ws = (char*)d_ws;
  (void)hipFuncSetAttribute((const void*)k1, hipFuncAttributeMaxDynamicSharedMemorySize, K1_LDS);
  (void)hipFuncSetAttribute((const void*)k2, hipFuncAttributeMaxDynamicSharedMemorySize, K2_LDS);
  (void)hipFuncSetAttribute((const void*)k3, hipFuncAttributeMaxDynamicSharedMemorySize, K3_LDS);
  k0<<<512 + 80 + 504, 256, 0, stream>>>(x, wqkv, wo, wg, wu, wd, ws);
  k1<<<256, 512, K1_LDS, stream>>>(x, wqkv, wg, wu, ws);
  k2<<<256, 512, K2_LDS, stream>>>(x, ws);
  k3<<<224, 512, K3_LDS, stream>>>(ws, (u16*)d_out);
}
```

```cpp
#include <hip/hip_runtime.h>
#include <hip/hip_bf16.h>
#include <stdint.h>

typedef __attribute__((ext_vector_type(8))) __bf16 bf16x8;
typedef __attribute__((ext_vector_type(2))) __bf16 bf16x2;
typedef __attribute__((ext_vector_type(4))) short s16x4;
typedef __attribute__((ext_vector_type(8))) short s16x8;
typedef __attribute__((ext_vector_type(2))) float f32x2;
typedef __attribute__((ext_vector_type(4))) float f32x4;
typedef __attribute__((ext_vector_type(16))) float f32x16;
typedef __attribute__((ext_vector_type(2))) unsigned u32x2;
typedef __attribute__((ext_vector_type(4))) unsigned u32x4;
typedef __attribute__((address_space(3))) char* ldsp;
typedef unsigned short u16;
typedef __amdgpu_buffer_rsrc_t rsrc_t;
#define DI __device__ __forceinline__
#define LDS(T, a) (*(__attribute__((address_space(3))) T*)(a))

constexpr int S = 2048, D = 448, DF = 1792;
constexpr unsigned WS_Q  = 0;
constexpr unsigned WS_KV = WS_Q + (2u << 20);
constexpr unsigned WS_H  = WS_KV + (4u << 20);
constexpr unsigned WS_O  = WS_H + 64 * 112 * 1024;
constexpr unsigned WS_ST = WS_O + (2u << 20);
constexpr unsigned WS_F1 = WS_ST + 2048 * 16;
constexpr unsigned WS_F2 = WS_F1 + 256 * 128;
constexpr unsigned WS_F3 = WS_F2 + 256 * 128;
constexpr unsigned WS_EP = WS_F3 + 256 * 128;
constexpr unsigned WS_END = WS_EP + 128;
constexpr unsigned OOB = 0xF0000000u;

DI unsigned pack2(float a, float b) {
  f32x2 v = {a, b};
  return __builtin_bit_cast(unsigned, __builtin_convertvector(v, bf16x2));
}
DI unsigned pack2(f32x2 v) { return __builtin_bit_cast(unsigned, __builtin_convertvector(v, bf16x2)); }
DI float lo16(unsigned v) { return __uint_as_float(v << 16); }
DI float hi16(unsigned v) { return __uint_as_float(v & 0xffff0000u); }
DI int rfl(int v) { return __builtin_amdgcn_readfirstlane(v); }
DI rsrc_t mkrsrc(const void* p, unsigned bytes) {
  return __builtin_amdgcn_make_buffer_rsrc((void*)p, (short)0, (int)bytes, 0x00020000);
}
DI void glds16(rsrc_t rs, unsigned voff, unsigned soff, unsigned ldsaddr) {
  asm volatile("s_mov_b32 m0, %0\n\ts_nop 0\n\tbuffer_load_dwordx4 %1, %2, %3 offen lds"
               :: "s"(ldsaddr), "v"(voff), "s"(rs), "s"(soff) : "m0", "memory");
}
DI void st16(rsrc_t rs, u32x4 v, unsigned voff, unsigned soff) {
  asm volatile("buffer_store_dwordx4 %0, %1, %2, %3 offen sc1\n\ts_nop 1" :: "v"(v), "v"(voff), "s"(rs), "s"(soff) : "memory");
}
DI bf16x8 ld16(rsrc_t rs, unsigned voff, unsigned soff) {
  return __builtin_bit_cast(bf16x8, __builtin_amdgcn_raw_buffer_load_b128(rs, voff, soff, 16));
}
DI void drain() { asm volatile("s_waitcnt vmcnt(0)" ::: "memory"); }
DI void arrive(ldsp cnt, int nwaves, rsrc_t rs, unsigned flag, unsigned tag, int l) {
  if (l == 0) {
    unsigned old = __hip_atomic_fetch_add((__attribute__((address_space(3))) unsigned*)cnt, 1u, __ATOMIC_RELAXED, __HIP_MEMORY_SCOPE_WORKGROUP);
    if (old == (unsigned)(nwaves - 1)) __builtin_amdgcn_raw_buffer_store_b32(tag, rs, flag, 0, 16);
  }
}
constexpr int SPIN_MAX = 1 << 18;
DI void poll(rsrc_t rs, unsigned voff, unsigned tag) {
#pragma unroll 1
  for (int it = 0; it < SPIN_MAX; it++) {
    unsigned v;
    asm volatile("buffer_load_dword %0, %1, %2, 0 offen sc1\n\ts_waitcnt vmcnt(0)" : "=v"(v) : "v"(voff), "s"(rs) : "memory");
    if (__builtin_amdgcn_ballot_w64(v != tag) == 0) break;
    __builtin_amdgcn_s_sleep(2);
  }
}
DI s16x4 trread(ldsp p) {
  return __builtin_amdgcn_ds_read_tr16_b64_v4i16((__attribute__((address_space(3))) s16x4*)p);
}
DI bf16x8 cat(s16x4 a, s16x4 b) {
  s16x8 r = __builtin_shufflevector(a, b, 0, 1, 2, 3, 4, 5, 6, 7);
  return __builtin_bit_cast(bf16x8, r);
}
template <int C> DI float dpp_add(float v) {
  return v + __int_as_float(__builtin_amdgcn_update_dpp(0, __float_as_int(v), C, 0xf, 0xf, true));
}
DI float wave_sum(float v) {
  v = dpp_add<0x111>(v); v = dpp_add<0x112>(v); v = dpp_add<0x114>(v); v = dpp_add<0x118>(v);
  const int i = __float_as_int(v);
  return (__int_as_float(__builtin_amdgcn_readlane(i, 15)) + __int_as_float(__builtin_amdgcn_readlane(i, 31))) +
         (__int_as_float(__builtin_amdgcn_readlane(i, 47)) + __int_as_float(__builtin_amdgcn_readlane(i, 63)));
}
#define MFMA16(a, b, c) __builtin_amdgcn_mfma_f32_16x16x32_bf16((a), (b), (c), 0, 0, 0)
#define MFMA32(a, b, c) __builtin_amdgcn_mfma_f32_32x32x16_bf16((a), (b), (c), 0, 0, 0)
#define SB() __builtin_amdgcn_sched_barrier(0)
#define WAITVM(n) asm volatile("s_waitcnt vmcnt(%0)" :: "n"(n) : "memory")
DI void bar() {
  __builtin_amdgcn_s_waitcnt(0xC07F);
  asm volatile("s_barrier" ::: "memory");
}

constexpr int WSLOT = 20480, NWS = 5, XRING = NWS * WSLOT, XSLOT = 16384;
constexpr int L_CS = XRING + 3 * XSLOT, L_ST = L_CS + 1536, L_CNT = L_ST + 1536, LDS_BYTES = L_CNT + 64;
constexpr int GRING = 24576, L_AX = 4 * GRING;

struct Sched { int n; int it[8]; };
constexpr Sched k1_sched(int t) {
  if (t == -1) return {6, {16, 17, 0, 1, 18, 19, 0, 0}};
  if (t == 0) return {3, {2, 3, 20, 0, 0, 0, 0, 0}};
  if (t == 1) return {4, {21, 4, 5, 22, 0, 0, 0, 0}};
  if (t == 2) return {3, {23, 6, 7, 0, 0, 0, 0, 0}};
  Sched s = {0, {0, 0, 0, 0, 0, 0, 0, 0}};
  int X = (t - 1) / 2 + 3;
  if (X < 7) s.it[s.n++] = 16 + 2 * X + ((t - 1) & 1);
  if (t + 5 < 14) s.it[s.n++] = t + 5;
  return s;
}
constexpr int k1_allow(int s, int nw) {
  int pos = 0, need = 0;
  const int st = s + 1, xs = (s + 1) >> 1;
  for (int t = -1; t < (s < 0 ? 0 : s); t++) {
    Sched c = k1_sched(t);
    for (int i = 0; i < c.n; i++) {
      int it = c.it[i];
      pos += it < 16 ? nw : 1;
      if (it == st || (it >= 16 && ((it - 16) >> 1) == xs)) need = pos;
    }
  }
  return pos - need;
}
#define STAG0 3

DI void issue_wd(rsrc_t rsD, unsigned voff, int pair, unsigned dst) {
#pragma unroll
  for (int i = 0; i < 4; i++) glds16(rsD, voff, (pair * 32 + i * 8) * 896, dst + i * 1024);
}

template <bool ISV>
DI unsigned phase1(const u16* x, const u16* wqkv, const u16* wg, const u16* wu, char* ws, rsrc_t rsW, rsrc_t rsD, unsigned voffD,
                   bool ffn_on, ldsp lds, int rb, int cb, int w, int l) {
  const unsigned lb = (unsigned)(size_t)lds;
  const int mh = w >> 2, n = w & 3, g = l >> 4, l15 = l & 15;
  constexpr int NT = ISV ? 4 : 5, NW0 = ISV ? 2 : 3;
  const rsrc_t rsX = mkrsrc(x, S * D * 2);
  const rsrc_t rsQ = mkrsrc(wqkv, D * 3 * D * 2);
  const rsrc_t rsG = ISV ? rsQ : mkrsrc(wg, D * DF * 2);
  const rsrc_t rsU = ISV ? rsQ : mkrsrc(wu, D * DF * 2);
  constexpr unsigned GP = ISV ? 2688 : 3584;
  const unsigned vcol = 896 + (cb - 14) * 192;
  const unsigned voffX = (rb * 128 + 8 * w + (l >> 3)) * 896 + (((l & 7) ^ ((4 * w + (l >> 4)) & 7)) << 4);
  const unsigned voffG = (4 * w + g) * GP + (ISV ? vcol * 2 : cb * 256) + ((l15 ^ ((g | ((w & 2) << 1)) << 1)) << 4);
  const unsigned voffP = (8 * w + (l >> 3)) * 2688 + cb * 128 + (((l & 7) ^ ((((l >> 4) & 1) | ((w & 1) << 1)) << 1)) << 4);
  auto issue = [&](int it, bool withp) {
    if (it >= 16) {
      const int X = (it - 16) >> 1, half = it & 1;
      glds16(rsX, voffX, half * 64 * 896 + X * 128, lb + XRING + (X % 3) * XSLOT + half * 8192 + w * 1024);
    } else {
      const unsigned d = lb + (it % NWS) * WSLOT + w * 1024;
      glds16(rsG, voffG, it * (32 * GP), d);
      glds16(rsU, voffG, it * (32 * GP) + (ISV ? 256 : 0), d + 8192);
      if (!ISV && withp) glds16(rsQ, voffP, it * (32 * 2688), d + 16384);
    }
  };
  const int srow = rb * 128 + cb * 8 + w;
  u32x4 xr;
  asm volatile("buffer_load_dwordx4 %0, %1, %2, %3 offen" : "=v"(xr) : "v"(l < 56 ? l * 16 : OOB), "s"(rsX), "s"(srow * 896) : "memory");
  {
    constexpr Sched c = k1_sched(-1);
#pragma unroll
    for (int i = 0; i < c.n; i++) issue(c.it[i], !mh);
  }
  unsigned tag = *(const __attribute__((address_space(4))) unsigned*)(ws + WS_EP);
  if (w == 0 && l < 4) LDS(unsigned, lds + L_CNT + l * 4) = 0;
  const int q = l15 >> 2, p = l & 3;
  const int ag = (8 * g + q) * 256 + (((2 * n) ^ (q | ((g & 1) << 2))) << 5) + p * 8;
  const int ag0 = ag ^ (mh << 5), ag1 = ag0 ^ 32;
  const int ap = 16384 + (8 * g + q) * 128 + ((n ^ ((q >> 1) | ((g & 1) << 1))) << 5) + p * 8;
  const int ax = XRING + (mh * 64 + l15) * 128 + ((g ^ (l15 >> 1)) << 4);

  f32x4 acc[NT][4], csa[3];
#pragma unroll
  for (int t = 0; t < NT; t++)
#pragma unroll
    for (int m = 0; m < 4; m++) acc[t][m] = (f32x4){0.f, 0.f, 0.f, 0.f};
#pragma unroll
  for (int t = 0; t < 3; t++) csa[t] = (f32x4){0.f, 0.f, 0.f, 0.f};
  bf16x8 ones = __builtin_bit_cast(bf16x8, (u32x4){0x3f803f80u, 0x3f803f80u, 0x3f803f80u, 0x3f803f80u});
  asm volatile("" : "+v"(ones));
  s16x4 wl[2][NT], wh[2][NT]; bf16x8 xf[2][4];
  auto rd = [&](int s, int r) {
    const int o = (s % NWS) * WSLOT, bs = s & 1, xo = ((s >> 1) % 3) * XSLOT;
    if (r < 4) xf[bs][r] = LDS(const bf16x8, lds + ((ax ^ (bs << 6)) + xo + r * 2048));
    else {
      const int t = (r - 4) >> 1, hi = (r - 4) & 1;
      if (t < NT) {
        const int a = (t == 4 ? ap + hi * 512 : ((t & 2) ? ag1 : ag0) + (t & 1) * 8192 + hi * 1024) + o;
        if (hi) wh[bs][t] = trread(lds + a); else wl[bs][t] = trread(lds + a);
      }
    }
  };
  auto stats = [&]() {
    asm volatile("" : "+v"(xr), "+s"(tag));
    tag += 1;
    float v[8], s = 0.f, qq = 0.f;
#pragma unroll
    for (int i = 0; i < 4; i++) { v[2 * i] = lo16(xr[i]); v[2 * i + 1] = hi16(xr[i]); s += v[2 * i] + v[2 * i + 1]; }
    const float mean = wave_sum(s) * (1.0f / D);
#pragma unroll
    for (int i = 0; i < 8; i++) { float d = v[i] - mean; qq += d * d; }
    const float var = wave_sum(l < 56 ? qq : 0.f) * (1.0f / D) + 1e-5f;
    u32x4 o = {__float_as_uint(mean), tag, __float_as_uint(var), tag};
    st16(rsW, o, l == 0 ? 0 : OOB, WS_ST + srow * 16);
  };
  u32x4 sg;
  const unsigned voffS = w < 2 ? WS_ST + (rb * 128 + w * 64 + l) * 16 : OOB;
  if (mh) WAITVM(k1_allow(-1, 2)); else WAITVM(k1_allow(-1, NW0));
  bar();
#pragma unroll
  for (int r = 0; r < 14; r++) rd(0, r);
  stats();
  if (mh) WAITVM(k1_allow(0, 2)); else WAITVM(k1_allow(0, NW0));
#define K1_STEP(s)                                                                          \
  {                                                                                         \
    if (s < 13) bar();                                                                      \
    constexpr Sched c = k1_sched(s);                                                        \
    if (s >= STAG0 && c.n > 0 && mh) { _Pragma("unroll") for (int i = 0; i < c.n; i++) issue(c.it[i], false); } \
    if (s == 12) asm volatile("buffer_load_dwordx4 %0, %1, %2, 0 offen sc1" : "=v"(sg) : "v"(voffS), "s"(rsW) : "memory"); \
    SB();                                                                                   \
    _Pragma("unroll") for (int i = 0; i < NT * 4; i++) {                                    \
      const int t = i >> 2, m = i & 3;                                                      \
      const bf16x8 wf = cat(wl[s & 1][t], wh[s & 1][t]);                                    \
      acc[t][m] = ISV ? MFMA16(xf[s & 1][m], wf, acc[t][m]) : MFMA16(wf, xf[s & 1][m], acc[t][m]); \
      if (m == 3 && t < 2) csa[t] = ISV ? MFMA16(ones, wf, csa[t]) : MFMA16(wf, ones, csa[t]); \
      if (s < 13 && i < 14) rd(s + 1, i);                                                   \
      if (s < STAG0 && (i & 3) == 1 && (i >> 2) < c.n) issue(c.it[i >> 2], !mh);            \
      SB();                                                                                 \
    }                                                                                       \
    if (mh) {                                                                               \
      if (!ISV && (s & 1)) csa[2] = MFMA16(cat(wl[s & 1][NT - 1], wh[s & 1][NT - 1]), ones, csa[2]); \
      SB(); if (s < 12) WAITVM(k1_allow(s < 12 ? s + 1 : 0, 2));                                  \
    } else {                                                                                \
      if (!ISV && !(s & 1)) csa[2] = MFMA16(cat(wl[s & 1][NT - 1], wh[s & 1][NT - 1]), ones, csa[2]); \
      SB(); if (s >= STAG0) { _Pragma("unroll") for (int i = 0; i < c.n; i++) issue(c.it[i], true); } \
      if (s < 12) WAITVM(k1_allow(s < 12 ? s + 1 : 0, NW0));                                \
    }                                                                                       \
    SB();                                                                                   \
  }
  K1_STEP(0) K1_STEP(1) K1_STEP(2) K1_STEP(3) K1_STEP(4) K1_STEP(5) K1_STEP(6)
  K1_STEP(7) K1_STEP(8) K1_STEP(9) K1_STEP(10) K1_STEP(11) K1_STEP(12) K1_STEP(13)

  asm volatile("s_waitcnt vmcnt(0)" : "+v"(sg) :: "memory");
  if (w < 2) {
#pragma unroll 1
    for (int it = 0; it < SPIN_MAX && __builtin_amdgcn_ballot_w64(sg[1] != tag || sg[3] != tag) != 0; it++) {
      __builtin_amdgcn_s_sleep(2);
      asm volatile("buffer_load_dwordx4 %0, %1, %2, 0 offen sc1\n\ts_waitcnt vmcnt(0)" : "=v"(sg) : "v"(voffS), "s"(rsW) : "memory");
    }
    const ldsp sp = lds + L_ST + (w * 64 + l) * 4;
    LDS(unsigned, sp) = sg[0]; LDS(unsigned, sp + 512) = sg[2];
    LDS(float, sp + 1024) = __builtin_amdgcn_rsqf(__uint_as_float(sg[2]));
  }
  {
    const ldsp cp = lds + L_CS + w * 192;
    if (ISV) { if (g == 0) { LDS(float, cp + l15 * 4) = csa[0][0]; LDS(float, cp + 64 + l15 * 4) = csa[1][0]; } }
    else if (l15 == 0) { LDS(f32x4, cp + g * 16) = csa[0]; LDS(f32x4, cp + 64 + g * 16) = csa[1]; LDS(f32x4, cp + 128 + g * 16) = csa[2]; }
  }
  bar();
  const ldsp cp = lds + L_CS + (w ^ 4) * 192;
  const int tt0 = rb * 4 + mh * 2;
  const unsigned f1 = WS_F1 + (rb * 16 + cb) * 128;
  if (ISV) {
    float cs[4] = {csa[0][0], csa[1][0], LDS(float, cp + l15 * 4), LDS(float, cp + 64 + l15 * 4)};
    f32x4 mean[4], rs[4];
#pragma unroll
    for (int m = 0; m < 4; m++) {
      const ldsp sp = lds + L_ST + (mh * 64 + m * 16 + g * 4) * 4;
      mean[m] = LDS(f32x4, sp); rs[m] = LDS(f32x4, sp + 1024);
    }
#pragma unroll
    for (int t = 0; t < 4; t++) {
      const int col = (cb - 14) * 192 + (t & 1) * 128 + (2 * n + ((t >> 1) ^ mh)) * 16 + l15;
      const int head = col / 28, d = col - head * 28;
      const unsigned vo = (head >> 3) == cb - 14 ? head * (64 * 4096) + (g >> 1) * 1024 + (d + 32 * (g & 1)) * 16 : OOB;
#pragma unroll
      for (int a = 0; a < 2; a++) {
        const f32x4 v0 = (acc[t][2 * a] - mean[2 * a] * cs[t]) * rs[2 * a], v1 = (acc[t][2 * a + 1] - mean[2 * a + 1] * cs[t]) * rs[2 * a + 1];
        u32x4 o = {pack2(v0[0], v0[1]), pack2(v0[2], v0[3]), pack2(v1[0], v1[1]), pack2(v1[2], v1[3])};
        st16(rsW, o, vo, WS_KV + 2048 + (tt0 + a) * 4096);
      }
    }
    {
      const unsigned v = (l & 3) == 0 ? 0x3f803f80u : 0u;
      u32x4 o = {v, v, v, v};
      st16(rsW, o, (l >> 4) * 4096 + ((l >> 3) & 1) * 1024 + (28 + (l & 3) + 32 * ((l >> 2) & 1)) * 16,
           WS_KV + 2048 + (((cb - 14) * 8 + w) * 64 + rb * 4) * 4096);
    }
    drain();
    arrive(lds + L_CNT, 8, rsW, f1, tag, l);
    if (mh && ffn_on) {
#pragma unroll
      for (int i = 0; i < 6; i++) issue_wd(rsD, voffD, i, lb + n * GRING + i * 4096);
    }
  } else {
    __builtin_amdgcn_s_setprio(1);
    f32x4 cs[5] = {csa[0], csa[1], LDS(f32x4, cp + g * 16), LDS(f32x4, cp + 64 + g * 16), csa[2] + LDS(f32x4, cp + 128 + g * 16)};
    float mean[4], var[4], rs[4];
#pragma unroll
    for (int m = 0; m < 4; m++) {
      const ldsp sp = lds + L_ST + (mh * 64 + m * 16 + l15) * 4;
      mean[m] = LDS(float, sp); var[m] = LDS(float, sp + 512); rs[m] = LDS(float, sp + 1024);
    }
    {
      const bool isq = cb < 7;
      const float sc = isq ? 0.18898223650461362f * 1.4426950408889634f : 1.0f;
      const int c = (isq ? cb : cb - 7) * 8 + 2 * n + (g >> 1), hp = c / 7, c7 = c - hp * 7;
      const int hs = isq ? 64 * 2048 : 64 * 4096, ts = isq ? 2048 : 4096;
      const int row = isq ? (g & 1) * 16 + l15 : ((l15 & 8) << 1) | ((g & 1) << 3) | (l15 & 7);
      const int sl = c7 & 3;
      const unsigned vo = (2 * hp + (c7 > 3)) * hs + (sl >> 1) * 1024 + (row + 32 * (sl & 1)) * 16;
      const unsigned so = (isq ? WS_Q : WS_KV) + tt0 * ts;
#pragma unroll
      for (int a = 0; a < 2; a++) {
        const f32x4 v0 = (acc[4][2 * a] - mean[2 * a] * cs[4]) * (rs[2 * a] * sc);
        const f32x4 v1 = (acc[4][2 * a + 1] - mean[2 * a + 1] * cs[4]) * (rs[2 * a + 1] * sc);
        const u32x2 e0 = __builtin_amdgcn_permlane16_swap(pack2(v0[0], v0[1]), pack2(v1[0], v1[1]), false, false);
        const u32x2 e1 = __builtin_amdgcn_permlane16_swap(pack2(v0[2], v0[3]), pack2(v1[2], v1[3]), false, false);
        const bool sh = c7 == 3;
        u32x4 o = {e0[0], e1[0], sh ? 0u : e0[1], sh ? 0u : e1[1]}, o2 = {0u, 0u, e0[1], e1[1]};
        st16(rsW, o, vo, so + a * ts);
        st16(rsW, o2, sh ? vo + hs : OOB, so + a * ts);
      }
    }
    __builtin_amdgcn_s_setprio(0);
    if (mh) {
      drain();
      arrive(lds + L_CNT, 8, rsW, f1, tag, l);
      if (ffn_on) {
#pragma unroll
        for (int i = 0; i < 6; i++) issue_wd(rsD, voffD, i, lb + n * GRING + i * 4096);
      }
    }
    auto geglu = [&](int m) {
      const float k1c = -2.0f * 0.7978845608028654f * 1.4426950408889634f * rs[m];
      const float k2c = k1c * 0.044715f * rs[m] * rs[m];
      u32x4 o;
#pragma unroll
      for (int j = 0; j < 4; j++) {
        const int tg = j & 2, i = (j & 1) * 2;
        const f32x2 a = (f32x2){acc[tg][m][i], acc[tg][m][i + 1]} - mean[m] * (f32x2){cs[tg][i], cs[tg][i + 1]};
        const f32x2 u = (f32x2){acc[tg + 1][m][i], acc[tg + 1][m][i + 1]} - mean[m] * (f32x2){cs[tg + 1][i], cs[tg + 1][i + 1]};
        const f32x2 z = a * (k2c * (a * a) + k1c);
        const f32x2 e = {__builtin_amdgcn_exp2f(z[0]), __builtin_amdgcn_exp2f(z[1])};
        const f32x2 dn = e * var[m] + var[m];
        const f32x2 r = {__builtin_amdgcn_rcpf(dn[0]), __builtin_amdgcn_rcpf(dn[1])};
        o[j] = pack2(a * u * r);
      }
      return o;
    };
    const unsigned ho = (g >> 1) * 1024 + (l15 + 32 * (g & 1)) * 16, hso = WS_H + (tt0 * 112 + cb * 8 + n * 2) * 1024;
    const u32x4 h0 = geglu(0), h1 = geglu(1);
    SB();
    if (!mh) {
      drain();
      arrive(lds + L_CNT, 8, rsW, f1, tag, l);
    }
    st16(rsW, h0, ho, hso);
    st16(rsW, h1, ho + 256, hso);
    st16(rsW, geglu(2), ho, hso + 112 * 1024);
    st16(rsW, geglu(3), ho + 256, hso + 112 * 1024);
  }
  return tag;
}

DI bf16x8 pack8(const f32x16& s, int o) {
  u32x4 r = {pack2(s[o], s[o + 1]), pack2(s[o + 2], s[o + 3]), pack2(s[o + 4], s[o + 5]), pack2(s[o + 6], s[o + 7])};
  return __builtin_bit_cast(bf16x8, r);
}
DI void xwrite(ldsp base, int ss, const f32x16 (&a)[4], int w4, int l) {
#pragma unroll
  for (int t = 1; t < 4; t++)
#pragma unroll
    for (int qd = 0; qd < 4; qd++) {
      f32x4 v = {a[t][4 * qd], a[t][4 * qd + 1], a[t][4 * qd + 2], a[t][4 * qd + 3]};
      LDS(f32x4, base + (w4 * ss + l * 16 + (t - 1) * 4096 + qd * 1024)) = v;
    }
}
DI f32x16 xread(ldsp base, int ss, const f32x16 (&a)[4], int w4, int l) {
  f32x16 r = a[0];
#pragma unroll
  for (int t = 1; t < 4; t++)
#pragma unroll
    for (int qd = 0; qd < 4; qd++) {
      f32x4 v = LDS(const f32x4, base + ((w4 ^ t) * ss + l * 16 + (t - 1) * 4096 + qd * 1024));
#pragma unroll
      for (int i = 0; i < 4; i++) r[4 * qd + i] += v[i];
    }
  return r;
}
DI float other_half(float v) {
  const u32x2 e = __builtin_amdgcn_permlane32_swap(__float_as_uint(v), __float_as_uint(v), false, false);
  return __uint_as_float(threadIdx.x & 32 ? e[0] : e[1]);
}

__global__ __launch_bounds__(512, 2) void fused(const u16* x, const u16* wqkv, const u16* wo, const u16* wg, const u16* wu,
                                                const u16* wd, u16* out, char* ws) {
  extern __shared__ __attribute__((aligned(16))) char smem[];
  const ldsp lds = (ldsp)smem;
  const unsigned lb = (unsigned)(size_t)lds;
  const int b = blockIdx.x, tid = threadIdx.x, l = tid & 63, w = rfl(tid >> 6), w4 = w & 3;
  const int xcd = b & 7, idx = b >> 3;
  const int rb = (xcd >> 2) * 8 + (idx >> 2), cb = (xcd & 3) * 4 + (idx & 3);
  const int head = xcd * 2 + (idx >> 4), qb = idx & 15;
  const int fr = xcd * 4 + idx / 7, fc = idx % 7;
  const bool ffn_on = idx < 28;
  const rsrc_t rsW = mkrsrc(ws, WS_END), rsD = mkrsrc(wd, DF * D * 2);
  const unsigned voffR = (l >> 3) * 896 + fc * 128 + (((l & 7) ^ (((l >> 4) & 1) << 2)) << 4);
  const unsigned voffD = voffR + w4 * (448 * 896);
  unsigned tag;
  if (cb >= 14) tag = phase1<true>(x, wqkv, wg, wu, ws, rsW, rsD, voffD, ffn_on, lds, rb, cb, w, l);
  else tag = phase1<false>(x, wqkv, wg, wu, ws, rsW, rsD, voffD, ffn_on, lds, rb, cb, w, l);

  f32x16 acc[4];
#pragma unroll
  for (int t = 0; t < 4; t++)
#pragma unroll
    for (int i = 0; i < 16; i++) acc[t][i] = 0.f;
  drain();
  if (cb < 14) arrive(lds + L_CNT + 4, 8, rsW, WS_F2 + (rb * 16 + cb) * 128, tag, l);
  if (w < 4) {
    __builtin_amdgcn_s_setprio(2);
    {
      const int c0 = 7 * (head >> 1) + 3 * (head & 1), k = l >> 2;
      const int fcb = k == 0 ? 7 + (c0 >> 3) : k == 1 ? 7 + ((c0 + 3) >> 3) : k == 2 ? 14 + (head >> 3) : (l & 1) ? (c0 >> 3) : ((c0 + 3) >> 3);
      const int frb = k < 3 ? 4 * w4 + (l & 3) : qb;
      poll(rsW, WS_F1 + (frb * 16 + fcb) * 128, tag);
    }
    const unsigned qso = WS_Q + (head * 64 + qb * 4) * 2048, kso = WS_KV + (head * 64 + w4 * 16) * 4096;
    const unsigned vl = l * 16;
    bf16x8 Q[4][2], kv[4][4];
    kv[0][0] = ld16(rsW, vl, kso); kv[0][1] = ld16(rsW, vl + 1024, kso);
#pragma unroll
    for (int t = 0; t < 4; t++) { Q[t][0] = ld16(rsW, vl, qso + (t ^ w4) * 2048); Q[t][1] = ld16(rsW, vl + 1024, qso + (t ^ w4) * 2048); }
    kv[0][2] = ld16(rsW, vl + 2048, kso); kv[0][3] = ld16(rsW, vl + 3072, kso);
#pragma unroll
    for (int t = 1; t < 3; t++)
#pragma unroll
      for (int i = 0; i < 4; i++) kv[t][i] = ld16(rsW, vl + i * 1024, kso + t * 4096);
    kv[3][3] = __builtin_bit_cast(bf16x8, (u32x4){0u, 0u, 0u, 0u});
    asm volatile("" : "+v"(acc[0]), "+v"(acc[1]), "+v"(acc[2]), "+v"(acc[3]), "+v"(kv[3][3]));
    f32x16 zero;
#pragma unroll
    for (int i = 0; i < 16; i++) zero[i] = 0.f;
#define AMF0(d, a, b) asm volatile("v_mfma_f32_32x32x16_bf16 %0, %1, %2, 0" : "=&v"(d) : "v"(a), "v"(b));
#define AMF(d, a, b) asm volatile("v_mfma_f32_32x32x16_bf16 %0, %1, %2, %0" : "+v"(d) : "v"(a), "v"(b));
#define EX(i) asm volatile("v_exp_f32 %0, %1" : "=v"(e[i]) : "v"(sc[i]));
#define CV(d, a, b) asm volatile("v_cvt_pk_bf16_f32 %0, %1, %2" : "=v"(d) : "v"(a), "v"(b));
    f32x16 sc;
    AMF0(sc, kv[0][0], Q[0][0]) AMF(sc, kv[0][1], Q[0][1])
    asm volatile("s_nop 15");
    float ep[4] = {0.f, 0.f, 0.f, 0.f};
    u32x4 ph = {0u, 0u, 0u, 0u};
    for (int ko = 0; ko < 4; ko++) {
      const unsigned vn = ko == 3 ? OOB : vl;
#pragma unroll
      for (int ki = 0; ki < 4; ki++) {
#pragma unroll
        for (int t = 0; t < 4; t++) {
          const int kn = t == 3 ? (ki + 1) & 3 : ki, tn = (t + 1) & 3, kp = t == 0 ? (ki + 3) & 3 : ki, tp = (t + 3) & 3;
          float e[16];
          f32x16 sn;
          u32x4 pl;
          AMF0(sn, kv[kn][0], Q[tn][0])
          EX(0) CV(ph[2], ep[0], ep[1]) EX(1) CV(ph[3], ep[2], ep[3]) EX(2) EX(3)
          AMF(sn, kv[kn][1], Q[tn][1])
          EX(4) CV(pl[0], e[0], e[1]) EX(5) CV(pl[1], e[2], e[3]) EX(6) EX(7)
          AMF(acc[tp], kv[kp][3], ph)
          EX(8) CV(pl[2], e[4], e[5]) EX(9) CV(pl[3], e[6], e[7]) EX(10) EX(11)
          AMF(acc[t], kv[ki][2], pl)
          SB();
          kv[(ki + 3) & 3][t] = ld16(rsW, (ki == 0 ? vl : vn) + t * 1024, kso + (ko * 4 + ki + 3) * 4096);
          SB();
          EX(12) CV(ph[0], e[8], e[9]) EX(13) CV(ph[1], e[10], e[11]) EX(14) EX(15)
#pragma unroll
          for (int i = 0; i < 4; i++) ep[i] = e[12 + i];
          sc = sn;
        }
      }
    }
    asm volatile("s_nop 1");
    CV(ph[2], ep[0], ep[1]) CV(ph[3], ep[2], ep[3])
    asm volatile("s_nop 1");
    AMF(acc[3], kv[3][3], ph)
    asm volatile("s_nop 15" : "+v"(acc[3]));
    xwrite(lds + L_AX, 12288, acc, w4, l);
    bar();
    f32x16 o = xread(lds + L_AX, 12288, acc, w4, l);
    float lsum = other_half(o[12]);
    lsum = l & 32 ? o[12] : lsum;
    {
      float am = 0.f;
#pragma unroll
      for (int i = 0; i < 16; i++) am += __builtin_fabsf(o[i]);
      const bool ok = lsum >= 0x1p-80f && lsum <= 0x1p80f && am < __builtin_inff();
      if (__builtin_amdgcn_ballot_w64(!ok) != 0) {
        const bf16x8 q0 = Q[0][0], q1 = Q[0][1];
        const unsigned ks = WS_KV + head * 64 * 4096;
        float mx = -__builtin_inff();
        for (int kt = 0; kt < 64; kt++) {
          f32x16 s = MFMA32(ld16(rsW, l * 16, ks + kt * 4096), q0, zero);
          s = MFMA32(ld16(rsW, l * 16 + 1024, ks + kt * 4096), q1, s);
#pragma unroll
          for (int i = 0; i < 16; i++) mx = __builtin_fmaxf(mx, s[i]);
        }
        mx = __builtin_fmaxf(mx, other_half(mx));
        o = zero;
        for (int kt = 0; kt < 64; kt++) {
          f32x16 s = MFMA32(ld16(rsW, l * 16, ks + kt * 4096), q0, zero);
          s = MFMA32(ld16(rsW, l * 16 + 1024, ks + kt * 4096), q1, s);
#pragma unroll
          for (int i = 0; i < 16; i++) s[i] = __builtin_amdgcn_exp2f(s[i] - mx);
          o = MFMA32(ld16(rsW, l * 16 + 2048, ks + kt * 4096), pack8(s, 0), o);
          o = MFMA32(ld16(rsW, l * 16 + 3072, ks + kt * 4096), pack8(s, 8), o);
        }
        lsum = other_half(o[12]);
        lsum = l & 32 ? o[12] : lsum;
      }
    }
    const float r = __builtin_amdgcn_rcpf(lsum), r3 = l & 32 ? 0.f : r;
#pragma unroll
    for (int i = 0; i < 16; i++) o[i] *= i < 12 ? r : r3;
    const unsigned oso = WS_O + ((qb * 4 + w4) * 32 + head * 2) * 1024;
    st16(rsW, __builtin_bit_cast(u32x4, pack8(o, 0)), l * 16, oso);
    st16(rsW, __builtin_bit_cast(u32x4, pack8(o, 8)), l * 16 + 1024, oso);
    drain();
    arrive(lds + L_CNT + 8, 4, rsW, WS_F3 + (qb * 16 + head) * 128, tag, l);
  } else if (ffn_on) {
    poll(rsW, WS_F2 + ((fr >> 1) * 16 + ((7 * w4) >> 1) + (l & 3)) * 128, tag);
    const unsigned hso = WS_H + (fr * 2 * 112 + w4 * 28) * 1024, ring = lb + w4 * GRING;
    bf16x8 hf[6][4];
    auto ldh = [&](int p) {
#pragma unroll
      for (int i = 0; i < 4; i++) hf[p % 6][i] = ld16(rsW, l * 16 + (i >> 1) * 1024, hso + p * 2048 + ((i & 1) ^ (w4 >> 1)) * (112 * 1024));
    };
#pragma unroll
    for (int p = 0; p < 6; p++) ldh(p);
    SB();
    const int i16 = l & 15;
    const int ab = w4 * GRING + (4 * (l >> 5) + (i16 >> 2)) * 128 + (((((l >> 4) & 1) * 2 + ((i16 & 3) >> 1)) ^ (((i16 >> 3) ^ (w4 & 1)) << 2)) << 4) + (i16 & 1) * 8;
    const int ad = ab + (fr & 1) * 2048;
    auto pair = [&](int p) {
      bf16x8 wf[4];
#pragma unroll
      for (int i = 0; i < 4; i++) {
        const int o = (p % 6) * 4096 + (i >> 1) * 1024;
        wf[i] = cat(trread(lds + ((ad ^ ((i & 1) << 6)) + o)), trread(lds + ((ad ^ ((i & 1) << 6) ^ 2048) + o)));
      }
#pragma unroll
      for (int i = 0; i < 8; i++) acc[i & 3] = MFMA32(wf[(i >> 2) * 2 + (i & 1)], hf[p % 6][(i >> 2) * 2 + ((i >> 1) & 1)], acc[i & 3]);
      SB();
    };
#pragma unroll
    for (int p = 0; p < 8; p++) {
      if (p >= 6) WAITVM(44);
      pair(p);
      issue_wd(rsD, voffD, p + 6, ring + (p % 6) * 4096);
      ldh(p + 6);
      SB();
    }
    const int tt = fr * 2 + (w4 >> 1), nt = fc * 2 + (w4 & 1);
    const unsigned xo = ((tt * 32 + (l & 31)) * D + nt * 32 + 8 * (l >> 5)) * 2;
    const u32x4 xa = *(const u32x4*)((const char*)x + xo), xb = *(const u32x4*)((const char*)x + xo + 32);
    drain();
    bar();
#pragma unroll
    for (int p = 8; p < 12; p++) pair(p);
    {
      const rsrc_t rsO = mkrsrc(wo, D * D * 2);
#pragma unroll
      for (int i = 0; i < 14; i++) glds16(rsO, voffR, (w4 * 112 + i * 8) * 896, ring + 8192 + i * 1024);
    }
    pair(12); pair(13);
    poll(rsW, WS_F3 + ((fr >> 1) * 16 + 4 * w4 + (l & 3)) * 128, tag);
    bf16x8 of[8][2];
#pragma unroll
    for (int s = 0; s < 8; s++)
#pragma unroll
      for (int i = 0; i < 2; i++) of[s][i] = ld16(rsW, l * 16 + (s & 3) * 1024, WS_O + ((fr * 2 + (i ^ (w4 >> 1))) * 32 + w4 * 8 + (s & 4)) * 1024);
    SB();
    bf16x8 wf[8][2];
#pragma unroll
    for (int s = 0; s < 8; s++)
#pragma unroll
      for (int i = 0; i < 2; i++) {
        const int o = 8192 + (28 * (s >> 1) + 16 * (s & 1)) * 128;
        wf[s][i] = cat(trread(lds + ((ab ^ (i << 6)) + o)), trread(lds + ((ab ^ (i << 6)) + o + 1024)));
      }
    SB();
#pragma unroll
    for (int s = 0; s < 8; s++)
#pragma unroll
      for (int i = 0; i < 4; i++) acc[i] = MFMA32(wf[s][i & 1], of[s][i >> 1], acc[i]);
    xwrite(lds, GRING, acc, w4, l);
    bar();
    const f32x16 o = xread(lds, GRING, acc, w4, l);
#pragma unroll
    for (int a = 0; a < 2; a++) {
      const u32x4 xv = a ? xb : xa;
      float v[8];
#pragma unroll
      for (int i = 0; i < 4; i++) {
        const u32x2 e = __builtin_amdgcn_permlane32_swap(__float_as_uint(o[8 * a + i]), __float_as_uint(o[8 * a + 4 + i]), false, false);
        v[i] = __uint_as_float(e[0]); v[4 + i] = __uint_as_float(e[1]);
      }
      u32x4 r;
#pragma unroll
      for (int i = 0; i < 4; i++) r[i] = pack2(v[2 * i] + lo16(xv[i]), v[2 * i + 1] + hi16(xv[i]));
      *(u32x4*)((char*)out + xo + a * 32) = r;
    }
  } else if (b == 255 && w == 4) {
#pragma unroll
    for (int i = 0; i < 4; i++) poll(rsW, WS_F1 + (i * 64 + l) * 128, tag);
    __builtin_amdgcn_raw_buffer_store_b32(tag, rsW, l == 0 ? WS_EP : OOB, 0, 16);
  }
}

extern "C" void kernel_launch(void* const* d_in, const int* in_sizes, int n_in, void* d_out, int out_size, void* d_ws,
                              size_t ws_size, hipStream_t stream) {
  (void)hipFuncSetAttribute((const void*)fused, hipFuncAttributeMaxDynamicSharedMemorySize, LDS_BYTES);
  fused<<<256, 512, LDS_BYTES, stream>>>((const u16*)d_in[0], (const u16*)d_in[1], (const u16*)d_in[2], (const u16*)d_in[4],
                                         (const u16*)d_in[5], (const u16*)d_in[6], (u16*)d_out, (char*)d_ws);
}
```

```cpp
#include <hip/hip_runtime.h>
#include <hip/hip_bf16.h>
#include <stdint.h>

typedef __attribute__((ext_vector_type(8))) __bf16 bf16x8;
typedef __attribute__((ext_vector_type(2))) __bf16 bf16x2;
typedef __attribute__((ext_vector_type(4))) short s16x4;
typedef __attribute__((ext_vector_type(8))) short s16x8;
typedef __attribute__((ext_vector_type(2))) float f32x2;
typedef __attribute__((ext_vector_type(4))) float f32x4;
typedef __attribute__((ext_vector_type(16))) float f32x16;
typedef __attribute__((ext_vector_type(2))) unsigned u32x2;
typedef __attribute__((ext_vector_type(4))) unsigned u32x4;
typedef __attribute__((ext_vector_type(16))) unsigned u32x16;
typedef __attribute__((ext_vector_type(32))) unsigned u32x32;
typedef __attribute__((address_space(3))) char* ldsp;
typedef unsigned short u16;
typedef __amdgpu_buffer_rsrc_t rsrc_t;
#define DI __device__ __forceinline__
#define LDS(T, a) (*(__attribute__((address_space(3))) T*)(a))

constexpr int S = 2048, D = 448, DF = 1792;
constexpr unsigned WS_Q  = 0;
constexpr unsigned WS_KV = WS_Q + (2u << 20);
constexpr unsigned WS_H  = WS_KV + (4u << 20);
constexpr unsigned WS_O  = WS_H + 64 * 112 * 1024;
constexpr unsigned WS_ST = WS_O + (2u << 20);
constexpr unsigned WS_F1 = WS_ST + 2048 * 16;
constexpr unsigned WS_F2 = WS_F1 + 256 * 128;
constexpr unsigned WS_F3 = WS_F2 + 256 * 128;
constexpr unsigned WS_EP = WS_F3 + 256 * 128;
constexpr unsigned WS_END = WS_EP + 128;
constexpr unsigned OOB = 0xF0000000u;

DI unsigned pack2(float a, float b) {
  f32x2 v = {a, b};
  return __builtin_bit_cast(unsigned, __builtin_convertvector(v, bf16x2));
}
DI unsigned pack2(f32x2 v) { return __builtin_bit_cast(unsigned, __builtin_convertvector(v, bf16x2)); }
DI float lo16(unsigned v) { return __uint_as_float(v << 16); }
DI float hi16(unsigned v) { return __uint_as_float(v & 0xffff0000u); }
DI int rfl(int v) { return __builtin_amdgcn_readfirstlane(v); }
DI rsrc_t mkrsrc(const void* p, unsigned bytes) {
  return __builtin_amdgcn_make_buffer_rsrc((void*)p, (short)0, (int)bytes, 0x00020000);
}
DI void glds16(rsrc_t rs, unsigned voff, unsigned soff, unsigned ldsaddr) {
  asm volatile("s_mov_b32 m0, %0\n\ts_nop 0\n\tbuffer_load_dwordx4 %1, %2, %3 offen lds"
               :: "s"(ldsaddr), "v"(voff), "s"(rs), "s"(soff) : "m0", "memory");
}
DI void st16(rsrc_t rs, u32x4 v, unsigned voff, unsigned soff) {
  asm volatile("buffer_store_dwordx4 %0, %1, %2, %3 offen sc1\n\ts_nop 1" :: "v"(v), "v"(voff), "s"(rs), "s"(soff) : "memory");
}
DI bf16x8 ld16(rsrc_t rs, unsigned voff, unsigned soff) {
  return __builtin_bit_cast(bf16x8, __builtin_amdgcn_raw_buffer_load_b128(rs, voff, soff, 16));
}
DI void drain() { asm volatile("s_waitcnt vmcnt(0)" ::: "memory"); }
DI void arrive(ldsp cnt, int nwaves, rsrc_t rs, unsigned flag, unsigned tag, int l) {
  unsigned old;
  asm volatile("ds_add_rtn_u32 %0, %1, %2\n\ts_waitcnt lgkmcnt(0)" : "=v"(old) : "v"((unsigned)(size_t)cnt + l * 4), "v"(1u) : "memory");
  __builtin_amdgcn_raw_buffer_store_b32(tag, rs, l == 0 && old == (unsigned)(nwaves - 1) ? flag : OOB, 0, 16);
}
constexpr int SPIN_MAX = 1 << 18;
DI void poll(rsrc_t rs, unsigned voff, unsigned tag) {
#pragma unroll 1
  for (int it = 0; it < SPIN_MAX; it++) {
    unsigned v;
    asm volatile("buffer_load_dword %0, %1, %2, 0 offen sc1\n\ts_waitcnt vmcnt(0)" : "=v"(v) : "v"(voff), "s"(rs) : "memory");
    if (__builtin_amdgcn_ballot_w64(v != tag) == 0) break;
    __builtin_amdgcn_s_sleep(2);
  }
}
DI s16x4 trread(ldsp p) {
  return __builtin_amdgcn_ds_read_tr16_b64_v4i16((__attribute__((address_space(3))) s16x4*)p);
}
DI bf16x8 cat(s16x4 a, s16x4 b) {
  s16x8 r = __builtin_shufflevector(a, b, 0, 1, 2, 3, 4, 5, 6, 7);
  return __builtin_bit_cast(bf16x8, r);
}
template <int C> DI float dpp_add(float v) {
  return v + __int_as_float(__builtin_amdgcn_update_dpp(0, __float_as_int(v), C, 0xf, 0xf, true));
}
DI float wave_sum(float v) {
  v = dpp_add<0x111>(v); v = dpp_add<0x112>(v); v = dpp_add<0x114>(v); v = dpp_add<0x118>(v);
  const int i = __float_as_int(v);
  return (__int_as_float(__builtin_amdgcn_readlane(i, 15)) + __int_as_float(__builtin_amdgcn_readlane(i, 31))) +
         (__int_as_float(__builtin_amdgcn_readlane(i, 47)) + __int_as_float(__builtin_amdgcn_readlane(i, 63)));
}
#define MFMA16(a, b, c) __builtin_amdgcn_mfma_f32_16x16x32_bf16((a), (b), (c), 0, 0, 0)
#define MFMA32(a, b, c) __builtin_amdgcn_mfma_f32_32x32x16_bf16((a), (b), (c), 0, 0, 0)
#define SB() __builtin_amdgcn_sched_barrier(0)
#define WAITVM(n) asm volatile("s_waitcnt vmcnt(%0)" :: "n"(n) : "memory")
DI void bar() {
  __builtin_amdgcn_s_waitcnt(0xC07F);
  asm volatile("s_barrier" ::: "memory");
}

#define PRIO_A 2
#define PRIO_B 0
constexpr int XS[4] = {32768, 16384, 0, 49152}, PS = 65536, GB = 98304, GS[4] = {GB + 32768, GB + 49152, GB, GB + 16384};
constexpr int L_CS = PS + 24576, L_ST = L_CS + 1536, L_C1 = L_ST + 1536, L_C3 = L_C1 + 256, L_W = L_C3 + 256, LDS_BYTES = 163840;
constexpr int RS[6] = {XS[3], XS[0], PS, GS[2], GS[3], XS[1]}, AX[3] = {XS[2], GS[0], GS[1]};
template <class T> DI T opaque(T v) { asm volatile("" : "+v"(v)); return v; }

struct Item { int kind, idx, j, need, early; };
struct Plan { Item it[17][2][8]; int n[17][2]; int allow[15][2]; int left; };
#define PQ1 8
#define PQ2 6
constexpr int ST_STEP = 1, SG_STEP = 7;
constexpr int k1_extra(bool isv, int o, int g) {
  if (o == 1) return 2;
  if (o == 3 + ST_STEP) return 1;
  if (o == 3 + SG_STEP) return g ? 0 : 1;
  if (o == 3 + 9) return g ? 4 : 0;
  if (o == 3 + 10) return (isv ? 0 : 4) + (g ? 4 : 0);
  if (o == 3 + 11 || o == 3 + 12) return g ? 8 : 0;
  return 0;
}
constexpr Plan k1_plan(bool isv) {
  Item st[100] = {};
  int n = 0;
  for (int b = -1; b <= 12; b++) {
    const int s = b + 1;
    if (!(s & 1) && (isv || s < 10)) for (int j = 0; j < 4; j++) st[n++] = {0, s / 2, j, b, s < 8 ? -9 : s - 7};
    for (int j = 0; j < 4; j++) st[n++] = {1 + (j & 1), s, j >> 1, b, s < 4 ? -9 : s - 4};
    if (!isv && s < 10) st[n++] = {3, s, 0, b, s < 6 ? -9 : s - 6};
    if (!isv && (b == 8 || b == 9)) {
      for (int j = 0; j < 4; j++) st[n++] = {0, b - 3, j, b, 2 * (b - 3) - 7};
      for (int j = 0; j < 2; j++) st[n++] = {3, 2 * b - 6 + j, 0, b, 2 * b - 12 + j};
    }
  }
  Plan p = {};
  int pos = 0;
  for (int o = 0; o < 3; o++) {
    const int q = o == 0 ? (isv ? 8 : 9) : o == 1 ? PQ1 : PQ2;
    for (int i = 0; i < q && st[pos].early < 0; i++) p.it[o][i & 1][p.n[o][i & 1]++] = st[pos++];
  }
  for (int s = 0; s < 14; s++)
    for (int g = 1; g >= 0; g--)
      for (int i = 0; i < 4 && pos < n && st[pos].early <= s; i++) p.it[3 + s][g][p.n[3 + s][g]++] = st[pos++];
  p.left = n - pos;
  for (int b = -1; b <= 12; b++)
    for (int g = 0; g < 2; g++) {
      int cnt = 0, last = 0;
      for (int o = 0; o < 17; o++) {
        for (int i = 0; i < p.n[o][g]; i++) {
          if (o <= b + 2) { cnt++; if (p.it[o][g][i].need <= b) last = cnt; }
          else if (p.it[o][g][i].need <= b) p.left = 99;
        }
        if (o <= b + 2) {
          cnt += k1_extra(isv, o, g);
          if ((o == 1 && b >= ST_STEP) || (o == 3 + SG_STEP && b >= 9)) last = cnt;
        }
      }
      p.allow[b + 1][g] = cnt - last;
    }
  return p;
}
constexpr Plan PLAN_R = k1_plan(false), PLAN_V = k1_plan(true);
static_assert(PLAN_R.left == 0 && PLAN_V.left == 0, "stream incomplete");

DI void gldsi(rsrc_t rs, unsigned voff, int soff, unsigned base, int off) {
  unsigned t;
  asm volatile("s_add_u32 m0, %1, %2\n\ts_mov_b32 %0, %3\n\tbuffer_load_dwordx4 %4, %5, %0 offen lds"
               : "=&s"(t) : "s"(base), "i"(off), "i"(soff), "v"(voff), "s"(rs) : "m0", "scc", "memory");
}
DI void issue_wd(rsrc_t rsD, unsigned voff, int pair, unsigned base, int slot) {
#pragma unroll
  for (int i = 0; i < 4; i++) gldsi(rsD, voff, (pair * 32 + i * 8) * 896, base, RS[slot] + i * 1024);
}
struct Epi { f32x4 acc[4][4], cs[4]; float mean[4], var[4], rs[4]; bool ready; };
DI int lane() {
  int l;
  asm volatile("v_mbcnt_lo_u32_b32 %0, -1, 0\n\tv_mbcnt_hi_u32_b32 %0, -1, %0" : "=v"(l));
  return l;
}
DI unsigned f1_of(int head, int qb, int w4, int l) {
  const int c0 = 7 * (head >> 1) + 3 * (head & 1), e = (l >> 2) & 7;
  const unsigned tab = (c0 >> 3) * 0x1001u + ((c0 + 3) >> 3) * 0x11110010u + ((head >> 3) << 8) + 0xe77u;
  return WS_F1 + ((e < 3 ? 4 * w4 + (l & 3) : qb) * 16 + ((tab >> (4 * e)) & 15)) * 128;
}

template <bool ISV, int mh>
DI unsigned phase1(const u16* x, const u16* wqkv, const u16* wg, const u16* wu, rsrc_t rsW, rsrc_t rsD, unsigned voffD,
                   ldsp lds, int rb, int cb, int head, int qb, int w, int l, Epi& ep) {
  const int n = w & 3, g = l >> 4, l15 = l & 15;
  const unsigned lb = (unsigned)(size_t)lds + n * 1024, lr = (unsigned)(size_t)lds + n * 4096;
  constexpr int NT = ISV ? 4 : 5;
  constexpr const Plan& P = ISV ? PLAN_V : PLAN_R;
  const rsrc_t rsX = mkrsrc(x, S * D * 2);
  const rsrc_t rsQ = mkrsrc(wqkv, D * 3 * D * 2);
  const rsrc_t rsG = ISV ? rsQ : mkrsrc(wg, D * DF * 2);
  const rsrc_t rsU = ISV ? rsQ : mkrsrc(wu, D * DF * 2);
  constexpr int GP = ISV ? 2688 : 3584;
  const unsigned vcol = 896 + (cb - 14) * 192;
  const unsigned voffX = (rb * 128 + 8 * n + (l >> 3)) * 896 + (((l & 7) ^ ((4 * n + (l >> 4)) & 7)) << 4);
  const unsigned voffG = (4 * n + g) * GP + (ISV ? vcol * 2 : cb * 256) + ((l15 ^ ((g | ((n & 2) << 1)) << 1)) << 4);
  const unsigned voffP = (8 * n + (l >> 3)) * 2688 + cb * 128 + (((l & 7) ^ ((((l >> 4) & 1) | ((n & 1) << 1)) << 1)) << 4);
  auto issue1 = [&](int kind, int idx, int j) {
    if (kind == 0) gldsi(rsX, voffX, j * 32 * 896 + idx * 128, lb, XS[idx & 3] + j * 4096);
    else if (kind == 1) gldsi(rsG, voffG, (idx * 32 + j * 16) * GP, lb, GS[idx & 3] + j * 4096);
    else if (kind == 2) gldsi(rsU, voffG, (idx * 32 + j * 16) * GP + (ISV ? 256 : 0), lb, GS[idx & 3] + 8192 + j * 4096);
    else gldsi(rsQ, voffP, idx * (32 * 2688), lb, PS + (idx % 6) * 4096);
  };
#define ISS1(o, g, i) if constexpr (i < P.n[o][g]) { constexpr Item t_ = P.it[o][g][i]; issue1(t_.kind, t_.idx, t_.j); }
#define ISSUE(o, g) { ISS1(o, g, 0) ISS1(o, g, 1) ISS1(o, g, 2) ISS1(o, g, 3) ISS1(o, g, 4) }
  if (mh) ISSUE(0, 1) else ISSUE(0, 0)
  asm volatile("s_barrier" ::: "memory");
  if (mh) ISSUE(1, 1) else ISSUE(1, 0)
  const int srow = rb * 128 + cb * 8 + w;
  u32x4 xr;
  unsigned tagv;
  asm volatile("buffer_load_dwordx4 %0, %1, %2, %3 offen" : "=v"(xr) : "v"(l < 56 ? l * 16 : OOB), "s"(rsX), "s"(srow * 896) : "memory");
  asm volatile("buffer_load_dword %0, off, %1, %2 sc1" : "=v"(tagv) : "s"(rsW), "s"(WS_EP) : "memory");
  unsigned tag = 0;
  LDS(u32x4, lds + L_C1 + l * 16) = (u32x4){0u, 0u, 0u, 0u};
  const int q = l15 >> 2, p = l & 3;
  const int ag = (8 * g + q) * 256 + (((2 * n) ^ (q | ((g & 1) << 2))) << 5) + p * 8;
  const ldsp ag0 = opaque(lds + GB + (ag ^ (mh << 5))), ag1 = opaque(lds + GB + (ag ^ (mh << 5) ^ 32));
  const ldsp ap = opaque(lds + PS + ((8 * g + q) * 128 + ((n ^ ((q >> 1) | ((g & 1) << 1))) << 5) + p * 8));
  const int ax = (mh * 64 + l15) * 128 + ((g ^ (l15 >> 1)) << 4);
  const ldsp ax0 = opaque(lds + ax), ax1 = opaque(lds + (ax ^ 64));
  const int tt0 = rb * 4 + mh * 2;
  const unsigned f1 = WS_F1 + (rb * 16 + cb) * 128;
  const bool isq = cb < 7;
  const int ts = isq ? 2048 : 4096;
  unsigned vo = 0, vo2 = 0;
  if (!ISV) {
    const int c = (isq ? cb : cb - 7) * 8 + 2 * n + (g >> 1), hp = c / 7, c7 = c - hp * 7;
    const int row = isq ? (g & 1) * 16 + l15 : ((l15 & 8) << 1) | ((g & 1) << 3) | (l15 & 7);
    const int sl = c7 & 3;
    vo = (2 * hp + (c7 > 3)) * (64 * ts) + (sl >> 1) * 1024 + (row + 32 * (sl & 1)) * 16;
    vo2 = c7 == 3 ? vo + 64 * ts : OOB;
    asm volatile("" : "+v"(vo), "+v"(vo2));
  }

  f32x4 acc[NT][4], csa[3];
#pragma unroll
  for (int t = 0; t < NT; t++)
#pragma unroll
    for (int m = 0; m < 4; m++) acc[t][m] = (f32x4){0.f, 0.f, 0.f, 0.f};
#pragma unroll
  for (int t = 0; t < 3; t++) csa[t] = (f32x4){0.f, 0.f, 0.f, 0.f};
  bf16x8 ones = __builtin_bit_cast(bf16x8, (u32x4){0x3f803f80u, 0x3f803f80u, 0x3f803f80u, 0x3f803f80u});
  asm volatile("" : "+v"(ones));
  s16x4 wl[2][NT], wh[2][NT], el[2], eh[2]; bf16x8 xf[2][4], xe[2][4];
  auto rdx = [&](int s, int r) { return LDS(const bf16x8, ((s & 1) ? ax1 : ax0) + (XS[(s >> 1) & 3] + r * 2048)); };
  auto rd = [&](int s, int r) {
    const int bs = s & 1;
    if (r < 4) xf[bs][r] = rdx(s, r);
    else {
      const int t = (r - 4) >> 1, hi = (r - 4) & 1;
      if (t < 4 || (t < NT && s < 10)) {
        const ldsp a = t == 4 ? ap + (hi * 512 + (s % 6) * 4096) : ((t & 2) ? ag1 : ag0) + ((t & 1) * 8192 + hi * 1024 + GS[s & 3] - GB);
        if (hi) wh[bs][t] = trread(a); else wl[bs][t] = trread(a);
      }
    }
  };
  auto rde = [&](int s, int r) {
    const int e = r / 6, k = 2 * s - 6 + e, i = r % 6;
    if (i < 4) xe[e][i] = rdx(k, i);
    else if (i == 4) el[e] = trread(ap + (k % 6) * 4096);
    else eh[e] = trread(ap + (512 + (k % 6) * 4096));
  };
  auto stats = [&]() {
    asm volatile("" : "+v"(xr), "+v"(tagv));
    tag = rfl(tagv) + 1;
    float v[8], s = 0.f, qq = 0.f;
#pragma unroll
    for (int i = 0; i < 4; i++) { v[2 * i] = lo16(xr[i]); v[2 * i + 1] = hi16(xr[i]); s += v[2 * i] + v[2 * i + 1]; }
    const float mean = wave_sum(s) * (1.0f / D);
#pragma unroll
    for (int i = 0; i < 8; i++) { float d = v[i] - mean; qq += d * d; }
    const float var = wave_sum(l < 56 ? qq : 0.f) * (1.0f / D) + 1e-5f;
    u32x4 o = {__float_as_uint(mean), tag, __float_as_uint(var), tag};
    st16(rsW, o, l == 0 ? 0 : OOB, WS_ST + srow * 16);
  };
  u32x4 sg;
  const unsigned voffS = w < 2 ? WS_ST + (rb * 128 + w * 64 + l) * 16 : OOB;
  auto pin = [&]() {
#pragma unroll
    for (int t = 0; t < 4; t++) asm volatile("" : "+v"(acc[t][0]), "+v"(acc[t][1]), "+v"(acc[t][2]), "+v"(acc[t][3]));
  };
  auto stats_in = [&]() {
    pin();
    asm volatile("" : "+v"(sg), "+v"(csa[0]), "+v"(csa[1]));
    if (!ISV) asm volatile("" : "+v"(acc[NT - 1][0]), "+v"(acc[NT - 1][1]), "+v"(acc[NT - 1][2]), "+v"(acc[NT - 1][3]), "+v"(csa[2]));
    if (w < 2) {
#pragma unroll 1
      for (int it = 0; it < SPIN_MAX && __builtin_amdgcn_ballot_w64(sg[1] != tag || sg[3] != tag) != 0; it++) {
        __builtin_amdgcn_s_sleep(2);
        asm volatile("buffer_load_dwordx4 %0, %1, %2, 0 offen sc1\n\ts_waitcnt vmcnt(0)" : "=v"(sg) : "v"(voffS), "s"(rsW) : "memory");
      }
      const ldsp sp = lds + L_ST + (w * 64 + l) * 4;
      LDS(unsigned, sp) = sg[0]; LDS(unsigned, sp + 512) = sg[2];
      LDS(float, sp + 1024) = __builtin_amdgcn_rsqf(__uint_as_float(sg[2]));
    }
  };
  const ldsp cpo = lds + L_CS + w * 192, cpp = lds + L_CS + (w ^ 4) * 192;
  f32x4 csp;
  auto qk_rd = [&]() {
    csp = LDS(f32x4, cpp + 128 + g * 16);
#pragma unroll
    for (int m = 0; m < 4; m++) {
      const ldsp sp = lds + L_ST + (mh * 64 + m * 16 + l15) * 4;
      ep.mean[m] = LDS(float, sp); ep.var[m] = LDS(float, sp + 512); ep.rs[m] = LDS(float, sp + 1024);
    }
  };
  auto qk_out = [&]() {
    const float sc = isq ? 0.18898223650461362f * 1.4426950408889634f : 1.0f;
    const unsigned so = (isq ? WS_Q : WS_KV) + tt0 * ts;
    float cs[4];
#pragma unroll
    for (int i = 0; i < 4; i++) { cs[i] = csa[2][i] + csp[i]; asm volatile("" : "+v"(cs[i])); }
#pragma unroll
    for (int a = 0; a < 2; a++) {
      float v[2][4];
#pragma unroll
      for (int j = 0; j < 2; j++) {
        const float r = ep.rs[2 * a + j] * sc;
#pragma unroll
        for (int i = 0; i < 4; i++) {
          v[j][i] = __builtin_fmaf(-ep.mean[2 * a + j], cs[i], acc[NT - 1][2 * a + j][i]);
          asm volatile("" : "+v"(v[j][i]));
          v[j][i] *= r;
          asm volatile("" : "+v"(v[j][i]));
        }
      }
      const u32x2 e0 = __builtin_amdgcn_permlane16_swap(pack2(v[0][0], v[0][1]), pack2(v[1][0], v[1][1]), false, false);
      const u32x2 e1 = __builtin_amdgcn_permlane16_swap(pack2(v[0][2], v[0][3]), pack2(v[1][2], v[1][3]), false, false);
      const bool sh = vo2 != OOB;
      u32x4 o = {e0[0], e1[0], sh ? 0u : e0[1], sh ? 0u : e1[1]}, o2 = {0u, 0u, e0[1], e1[1]};
      st16(rsW, o, vo, so + a * ts);
      st16(rsW, o2, vo2, so + a * ts);
    }
  };
  if (mh) WAITVM(P.allow[0][1]); else WAITVM(P.allow[0][0]);
  bar();
#pragma unroll
  for (int r = 0; r < 14; r++) rd(0, r);
  if (mh) { ISSUE(2, 1) WAITVM(P.allow[1][1]); } else { ISSUE(2, 0) WAITVM(P.allow[1][0]); }
#define K1_STEP(s)                                                                          \
  {                                                                                         \
    bar();                                                                                  \
    constexpr bool ext = !ISV && (s == 8 || s == 9);                                        \
    constexpr int nm = (ISV || s >= 10) ? 16 : ext ? 28 : 20;                               \
    if (!ISV && s == 10) qk_rd();                                                           \
    if (mh) {                                                                               \
      ISSUE(3 + s, 1)                                                                       \
      if (!ISV && s == 10) qk_out();                                                        \
      if (s == 9 || s == 10) issue_wd(rsD, voffD, s - 9, lr, s - 9);                        \
      if (s == 11 || s == 12) { issue_wd(rsD, voffD, 2 * s - 20, lr, 2 * s - 20); issue_wd(rsD, voffD, 2 * s - 19, lr, 2 * s - 19); } \
    }                                                                                       \
    SB();                                                                                   \
    _Pragma("unroll") for (int i = 0; i < nm; i++) {                                        \
      const int t = i >> 2, m = i & 3;                                                      \
      if (i < 20) {                                                                         \
        const bf16x8 wf = cat(wl[s & 1][t], wh[s & 1][t]);                                  \
        acc[t][m] = ISV ? MFMA16(xf[s & 1][m], wf, acc[t][m]) : MFMA16(wf, xf[s & 1][m], acc[t][m]); \
        if (m == 3 && t < 2) csa[t] = ISV ? MFMA16(ones, wf, csa[t]) : MFMA16(wf, ones, csa[t]); \
      } else acc[NT - 1][m] = MFMA16(cat(el[t - 5], eh[t - 5]), xe[t - 5][m], acc[NT - 1][m]); \
      if (ext) { if (i < 12) rde(s, i); else if (i < 26) rd(s + 1, i - 12); }               \
      else if (i < 14) rd(s + 1, i);                                                        \
      SB();                                                                                 \
    }                                                                                       \
    if (mh) {                                                                               \
      if (!ISV && s < 10 && (s & 1)) csa[2] = MFMA16(cat(wl[s & 1][NT - 1], wh[s & 1][NT - 1]), ones, csa[2]); \
      if (ext) csa[2] = MFMA16(cat(el[1], eh[1]), ones, csa[2]);                            \
      SB();                                                                                 \
    } else {                                                                                \
      if (!ISV && s < 10 && !(s & 1)) csa[2] = MFMA16(cat(wl[s & 1][NT - 1], wh[s & 1][NT - 1]), ones, csa[2]); \
      if (ext) csa[2] = MFMA16(cat(el[0], eh[0]), ones, csa[2]);                            \
      SB();                                                                                 \
      ISSUE(3 + s, 0)                                                                       \
      if (!ISV && s == 10) qk_out();                                                        \
    }                                                                                       \
    if (s == ST_STEP) stats();                                                              \
    if (s == SG_STEP && !mh) asm volatile("buffer_load_dwordx4 %0, %1, %2, 0 offen sc1" : "=v"(sg) : "v"(voffS), "s"(rsW) : "memory"); \
    if (s == 9 && !mh) stats_in();                                                                 \
    if (s < 12) { if (mh) WAITVM(P.allow[s < 12 ? s + 2 : 0][1]); else WAITVM(P.allow[s < 12 ? s + 2 : 0][0]); } \
    if (!ISV && s == 12) { if (mh) WAITVM(20); else drain(); arrive(lds + L_C1, 8, rsW, f1, tag, l); } \
    if (!ISV && s == 9) LDS(f32x4, cpo + 128 + g * 16) = csa[2];                            \
    SB();                                                                                   \
  }
  K1_STEP(0) K1_STEP(1) K1_STEP(2) K1_STEP(3) K1_STEP(4) K1_STEP(5) K1_STEP(6)
  K1_STEP(7) K1_STEP(8) K1_STEP(9) K1_STEP(10) K1_STEP(11) K1_STEP(12)
  const unsigned voffF = !ISV && !mh ? opaque(f1_of(head, qb, n, l)) : 0u;
#pragma unroll
  for (int t = 0; t < 2; t++) csa[t] = ISV ? MFMA16(ones, cat(wl[1][t], wh[1][t]), csa[t]) : MFMA16(cat(wl[1][t], wh[1][t]), ones, csa[t]);
  SB();
#pragma unroll
  for (int m = 0; m < 4; m++) acc[0][m] = ISV ? MFMA16(xf[1][m], cat(wl[1][0], wh[1][0]), acc[0][m]) : MFMA16(cat(wl[1][0], wh[1][0]), xf[1][m], acc[0][m]);
  SB();
  if (ISV) { LDS(float, cpo + l15 * 4) = csa[0][0]; LDS(float, cpo + 64 + l15 * 4) = csa[1][0]; }
  else { LDS(f32x4, cpo + g * 16) = csa[0]; LDS(f32x4, cpo + 64 + g * 16) = csa[1]; }
  bar();
  if (ISV) {
    float cs[4] = {csa[0][0], csa[1][0], LDS(float, cpp + l15 * 4), LDS(float, cpp + 64 + l15 * 4)};
    f32x4 mean[4], rs[4];
#pragma unroll
    for (int m = 0; m < 4; m++) {
      const ldsp sp = lds + L_ST + (mh * 64 + m * 16 + g * 4) * 4;
      mean[m] = LDS(f32x4, sp); rs[m] = LDS(f32x4, sp + 1024);
    }
    SB();
#pragma unroll
    for (int i = 4; i < 16; i++) acc[i >> 2][i & 3] = MFMA16(xf[1][i & 3], cat(wl[1][i >> 2], wh[1][i >> 2]), acc[i >> 2][i & 3]);
    SB();
    pin();
#pragma unroll
    for (int t = 0; t < 4; t++) {
      const int col = (cb - 14) * 192 + (t & 1) * 128 + (2 * n + ((t >> 1) ^ mh)) * 16 + l15;
      const int head = col / 28, d = col - head * 28;
      const unsigned vv = (head * (64 * 4096) + (g >> 1) * 1024 + (d + 32 * (g & 1)) * 16) | ((head >> 3) == cb - 14 ? 0u : OOB);
#pragma unroll
      for (int a = 0; a < 2; a++) {
        const f32x4 v0 = (acc[t][2 * a] - mean[2 * a] * cs[t]) * rs[2 * a], v1 = (acc[t][2 * a + 1] - mean[2 * a + 1] * cs[t]) * rs[2 * a + 1];
        u32x4 o = {pack2(v0[0], v0[1]), pack2(v0[2], v0[3]), pack2(v1[0], v1[1]), pack2(v1[2], v1[3])};
        st16(rsW, o, vv, WS_KV + 2048 + (tt0 + a) * 4096);
      }
    }
    {
      const unsigned v = (l & 3) == 0 ? 0x3f803f80u : 0u;
      u32x4 o = {v, v, v, v};
      st16(rsW, o, (l >> 4) * 4096 + ((l >> 3) & 1) * 1024 + (28 + (l & 3) + 32 * ((l >> 2) & 1)) * 16,
           WS_KV + 2048 + (((cb - 14) * 8 + w) * 64 + rb * 4) * 4096);
    }
    drain();
    arrive(lds + L_C1, 8, rsW, f1, tag, l);
    ep.ready = false;
  } else {
    unsigned pv;
    if (!mh) asm volatile("buffer_load_dword %0, %1, %2, 0 offen sc1" : "=v"(pv) : "v"(voffF), "s"(rsW) : "memory");
    ep.cs[0] = csa[0]; ep.cs[1] = csa[1]; ep.cs[2] = LDS(f32x4, cpp + g * 16); ep.cs[3] = LDS(f32x4, cpp + 64 + g * 16);
    SB();
#pragma unroll
    for (int i = 4; i < 16; i++) acc[i >> 2][i & 3] = MFMA16(cat(wl[1][i >> 2], wh[1][i >> 2]), xf[1][i & 3], acc[i >> 2][i & 3]);
    SB();
    pin();
#pragma unroll
    for (int i = 0; i < 16; i++) ep.acc[i >> 2][i & 3] = acc[i >> 2][i & 3];
    if (!mh) {
      asm volatile("s_waitcnt vmcnt(0)" : "+v"(pv) :: "memory");
      ep.ready = __builtin_amdgcn_ballot_w64(pv != tag) == 0;
    }
  }
  return tag;
}

DI void geglu_out(const Epi& ep, rsrc_t rsW, int rb, int cb, int w, int l) {
  const int g = l >> 4, l15 = l & 15;
  const unsigned ho = (g >> 1) * 1024 + (l15 + 32 * (g & 1)) * 16, hso = WS_H + ((rb * 4 + (w >> 2) * 2) * 112 + cb * 8 + (w & 3) * 2) * 1024;
#pragma unroll
  for (int m = 0; m < 4; m++) {
    const float k1c = -2.0f * 0.7978845608028654f * 1.4426950408889634f * ep.rs[m];
    const float k2c = k1c * 0.044715f * ep.rs[m] * ep.rs[m];
    u32x4 o;
#pragma unroll
    for (int j = 0; j < 4; j++) {
      const int tg = j & 2, i = (j & 1) * 2;
      const f32x2 a = (f32x2){ep.acc[tg][m][i], ep.acc[tg][m][i + 1]} - ep.mean[m] * (f32x2){ep.cs[tg][i], ep.cs[tg][i + 1]};
      const f32x2 u = (f32x2){ep.acc[tg + 1][m][i], ep.acc[tg + 1][m][i + 1]} - ep.mean[m] * (f32x2){ep.cs[tg + 1][i], ep.cs[tg + 1][i + 1]};
      const f32x2 z = a * (k2c * (a * a) + k1c);
      const f32x2 e = {__builtin_amdgcn_exp2f(z[0]), __builtin_amdgcn_exp2f(z[1])};
      const f32x2 dn = e * ep.var[m] + ep.var[m];
      const f32x2 r = {__builtin_amdgcn_rcpf(dn[0]), __builtin_amdgcn_rcpf(dn[1])};
      o[j] = pack2(a * u * r);
    }
    st16(rsW, o, ho + (m & 1) * 256, hso + (m >> 1) * (112 * 1024));
  }
}

DI bf16x8 pack8(const f32x16& s, int o) {
  u32x4 r = {pack2(s[o], s[o + 1]), pack2(s[o + 2], s[o + 3]), pack2(s[o + 4], s[o + 5]), pack2(s[o + 6], s[o + 7])};
  return __builtin_bit_cast(bf16x8, r);
}
DI void xwrite(ldsp base, const int* sl, const f32x16 (&a)[4], int w4, int l) {
#pragma unroll
  for (int t = 1; t < 4; t++)
#pragma unroll
    for (int qd = 0; qd < 4; qd++) {
      f32x4 v = {a[t][4 * qd], a[t][4 * qd + 1], a[t][4 * qd + 2], a[t][4 * qd + 3]};
      LDS(f32x4, base + (w4 * 4096 + l * 16 + sl[t - 1] + qd * 1024)) = v;
    }
}
DI f32x16 xread(ldsp base, const int* sl, const f32x16 (&a)[4], int w4, int l) {
  f32x16 r = a[0];
#pragma unroll
  for (int t = 1; t < 4; t++)
#pragma unroll
    for (int qd = 0; qd < 4; qd++) {
      f32x4 v = LDS(const f32x4, base + ((w4 ^ t) * 4096 + l * 16 + sl[t - 1] + qd * 1024));
#pragma unroll
      for (int i = 0; i < 4; i++) r[4 * qd + i] += v[i];
    }
  return r;
}
DI void lds_wait(ldsp a, int n, int l, int slp) {
#pragma unroll 1
  for (int it = 0; it < SPIN_MAX; it++) {
    unsigned v;
    asm volatile("ds_read_b32 %0, %1\n\ts_waitcnt lgkmcnt(0)" : "=v"(v) : "v"((unsigned)(size_t)a + (l & (n - 1)) * 4) : "memory");
    if (__builtin_amdgcn_ballot_w64(v == 0) == 0) break;
    if (slp) __builtin_amdgcn_s_sleep(1);
  }
}
DI void lds_set(ldsp a) {
  asm volatile("s_waitcnt lgkmcnt(0)\n\tds_write_b32 %0, %1" :: "v"((unsigned)(size_t)a), "v"(1u) : "memory");
}
DI float other_half(float v) {
  const u32x2 e = __builtin_amdgcn_permlane32_swap(__float_as_uint(v), __float_as_uint(v), false, false);
  return __uint_as_float(threadIdx.x & 32 ? e[0] : e[1]);
}

__global__ __launch_bounds__(512, 2) void fused(const u16* x, const u16* wqkv, const u16* wo, const u16* wg, const u16* wu,
                                                const u16* wd, u16* out, char* ws) {
  extern __shared__ __attribute__((aligned(16))) char smem[];
  const ldsp lds = (ldsp)smem;
  const unsigned lb = (unsigned)(size_t)lds;
  const int b = blockIdx.x, tid = threadIdx.x, l = tid & 63, w = rfl(tid >> 6), w4 = w & 3;
  const int xcd = b & 7, idx = b >> 3;
  const int rb = (xcd >> 2) * 8 + (idx >> 2);
  const int cb = (idx & 3) < 3 ? (xcd & 3) * 3 + (idx & 3) : (((idx >> 4) ^ xcd) & 1) * 2 + 12 + ((xcd >> 1) & 1);
  const int head = xcd * 2 + (idx >> 4), qb = idx & 15;
  const int fr = xcd * 4 + idx / 7, fc = idx % 7;
  const rsrc_t rsW = mkrsrc(ws, WS_END), rsD = mkrsrc(wd, DF * D * 2);
  const unsigned voffR = (l >> 3) * 896 + fc * 128 + (((l & 7) ^ (((l >> 4) & 1) << 2)) << 4);
  const unsigned voffD = voffR + w4 * (448 * 896);
  unsigned tag;
  Epi ep;
  f32x16 acc[4];
  if (w < 4) {
    if (cb >= 14) tag = phase1<true, 0>(x, wqkv, wg, wu, rsW, rsD, voffD, lds, rb, cb, head, qb, w, l, ep);
    else tag = phase1<false, 0>(x, wqkv, wg, wu, rsW, rsD, voffD, lds, rb, cb, head, qb, w, l, ep);
    const int l = lane();
    if (!ep.ready) poll(rsW, f1_of(head, qb, w4, l), tag);
    const unsigned qso = WS_Q + (head * 64 + qb * 4) * 2048, kso = WS_KV + (head * 64 + w4 * 16) * 4096;
    const unsigned vl = l * 16;
    u32x32 rq, rk0;
    u32x16 rk1;
#define LDV(r, s, o) "buffer_load_dwordx4 v[" #r ":" #r "+3], %4, %3, " s " offen offset:" #o " sc1\n\t"
    asm volatile(LDV(160, "%5", 0) LDV(164, "%5", 1024) LDV(224, "%8", 0) LDV(228, "%8", 1024)
                 LDV(232, "%9", 0) LDV(236, "%9", 1024) LDV(168, "%5", 2048) LDV(240, "%10", 0)
                 LDV(244, "%10", 1024) LDV(172, "%5", 3072) LDV(248, "%11", 0) LDV(252, "%11", 1024)
                 LDV(176, "%6", 0) LDV(180, "%6", 1024) LDV(184, "%6", 2048) LDV(188, "%6", 3072)
                 LDV(192, "%7", 0) LDV(196, "%7", 1024) LDV(200, "%7", 2048) LDV(204, "%7", 3072)
                 : "=&{v[224:255]}"(rq), "=&{v[160:191]}"(rk0), "=&{v[192:207]}"(rk1)
                 : "s"(rsW), "v"(vl), "s"(kso), "s"(kso + 4096), "s"(kso + 8192), "s"(qso + w4 * 2048),
                   "s"(qso + (w4 ^ 1) * 2048), "s"(qso + (w4 ^ 2) * 2048), "s"(qso + (w4 ^ 3) * 2048) : "memory");
    if (cb < 14) { geglu_out(ep, rsW, rb, cb, w, l); WAITVM(12); } else WAITVM(8);
    __builtin_amdgcn_s_setprio(PRIO_A);
    {
      u32x4 rsk = {(unsigned)(size_t)ws, (unsigned)((size_t)ws >> 32) & 0xffffu, WS_END, 0x00020000u};
      unsigned ko = kso;
#define R4(b) "v[" #b ":" #b "+3]"
#define R16(b) "v[" #b ":" #b "+15]"
#define MF(d, a, b, c) "v_mfma_f32_32x32x16_bf16 " R16(d) ", " R4(a) ", " R4(b) ", " c "\n\t"
#define EX(d, s) "v_exp_f32_e32 v[" #d "], v[" #s "]\n\t"
#define EXL(d, s) "v_exp_f32_e64 v[" #d "], v[" #s "]\n\t"
#define CV(d, a) "v_cvt_pk_bf16_f32 v[" #d "], v[" #a "], v[" #a "+1]\n\t"
#define UNIT(E0, SC, SN, KN, QN, AP, VP, AT, VT, LR, LV, LO, POST)                                          \
  MF(SN, KN, QN, "0") E0(SC, SC) EX(SC+1, SC+1) CV(58, 60) CV(59, 62) EX(SC+2, SC+2) EX(SC+3, SC+3)         \
  MF(SN, KN+4, QN+4, R16(SN)) EX(SC+4, SC+4) EX(SC+5, SC+5) CV(52, SC) CV(53, SC+2) EX(SC+6, SC+6) EX(SC+7, SC+7) \
  MF(AP, VP, 56, R16(AP)) EX(SC+8, SC+8) EX(SC+9, SC+9) CV(54, SC+4) CV(55, SC+6) EX(SC+10, SC+10) EX(SC+11, SC+11) \
  MF(AT, VT, 52, R16(AT)) "buffer_load_dwordx4 " R4(LR) ", v" #LV ", s[64:67], s68 offen offset:" #LO " sc1\n\t" POST \
  EX(60, SC+12) EX(61, SC+13) CV(56, SC+8) CV(57, SC+10) EX(62, SC+14) EX(63, SC+15)
#define TILE(K, KN, KP, LV, PRE3, POST3)                                                                    \
  UNIT(EX, 64, 80, K, 232, 144, KP+12, 96, K+8, KP, LV, 0, "")                                              \
  UNIT(EX, 80, 64, K, 240, 96, K+12, 112, K+8, KP+4, LV, 1024, "")                                          \
  UNIT(EX, 64, 80, K, 248, 112, K+12, 128, K+8, KP+8, LV, 2048, "")                                         \
  "s_waitcnt vmcnt(7)\n\t" PRE3                                                                             \
  UNIT(EXL, 80, 64, KN, 224, 128, K+12, 144, K+8, KP+12, LV, 3072, POST3)
      asm volatile(
          "v_mov_b32 v220, 0\n\tv_mov_b32 v221, 0\n\tv_mov_b32 v222, 0\n\tv_mov_b32 v223, 0\n\t"
          MF(64, 160, 224, "0") MF(64, 164, 228, R16(64))
          MF(96, 220, 220, "0") "v_mov_b32 v56, 0\n\tv_mov_b32 v57, 0\n\tv_mov_b32 v60, 0\n\tv_mov_b32 v61, 0\n\t"
          MF(112, 220, 220, "0") "v_mov_b32 v62, 0\n\tv_mov_b32 v63, 0\n\tv_add_u32 v49, 0x1000, v48\n\tv_add_u32 v50, 0x2000, v48\n\t"
          MF(128, 220, 220, "0") "v_add_u32 v51, 0x3000, v48\n\ts_add_u32 s69, s68, 0x10000\n\t"
          MF(144, 220, 220, "0")
          ".p2align 3\n"
          "1:\n\t"
          TILE(160, 176, 208, 51, "", "s_add_u32 s68, s68, 0x4000\n\ts_cmp_lg_u32 s68, s69\n\ts_cselect_b32 s66, s66, 0\n\t")
          TILE(176, 192, 160, 48, "", "")
          TILE(192, 208, 176, 49, "ds_write_b32 v47, v47\n\t", "")
          TILE(208, 160, 192, 50, "", "")
          "s_cbranch_scc1 1b\n\t"
          "s_nop 0\n\t" CV(58, 60) CV(59, 62) "s_nop 1\n\t" MF(144, 220, 56, R16(144))
          "s_waitcnt vmcnt(0)\n\ts_nop 15"
          : "=&{v[96:111]}"(acc[0]), "=&{v[112:127]}"(acc[1]), "=&{v[128:143]}"(acc[2]), "=&{v[144:159]}"(acc[3]),
            "+{v[160:191]}"(rk0), "+{v[192:207]}"(rk1), "+{s[64:67]}"(rsk), "+{s68}"(ko)
          : "{v[224:255]}"(rq), "{v48}"(vl), "{v47}"(lb + L_W + 32 + w * 4)
          : "memory", "scc", "s69", "v49", "v50", "v51", "v52", "v53", "v54", "v55", "v56", "v57", "v58", "v59", "v60", "v61", "v62", "v63",
            "v64", "v65", "v66", "v67", "v68", "v69", "v70", "v71", "v72", "v73", "v74", "v75", "v76", "v77", "v78", "v79",
            "v80", "v81", "v82", "v83", "v84", "v85", "v86", "v87", "v88", "v89", "v90", "v91", "v92", "v93", "v94", "v95",
            "v208", "v209", "v210", "v211", "v212", "v213", "v214", "v215", "v216", "v217", "v218", "v219", "v220", "v221", "v222", "v223");
    }
    xwrite(lds, AX, acc, w4, l);
    lds_set(lds + L_W + 16 + w4 * 4);
    lds_wait(lds + L_W + 16, 4, l, 0);
    f32x16 o = xread(lds, AX, acc, w4, l);
    float lsum = other_half(o[12]);
    lsum = l & 32 ? o[12] : lsum;
    {
      float am = 0.f;
#pragma unroll
      for (int i = 0; i < 16; i++) am += __builtin_fabsf(o[i]);
      const bool ok = lsum >= 0x1p-80f && lsum <= 0x1p80f && am < __builtin_inff();
      if (__builtin_amdgcn_ballot_w64(!ok) != 0) {
        const bf16x8 q0 = ld16(rsW, vl, qso + w4 * 2048), q1 = ld16(rsW, vl + 1024, qso + w4 * 2048);
        f32x16 zero;
#pragma unroll
        for (int i = 0; i < 16; i++) zero[i] = 0.f;
        const unsigned ks = WS_KV + head * 64 * 4096;
        float mx = -__builtin_inff();
        for (int kt = 0; kt < 64; kt++) {
          f32x16 s = MFMA32(ld16(rsW, l * 16, ks + kt * 4096), q0, zero);
          s = MFMA32(ld16(rsW, l * 16 + 1024, ks + kt * 4096), q1, s);
#pragma unroll
          for (int i = 0; i < 16; i++) mx = __builtin_fmaxf(mx, s[i]);
        }
        mx = __builtin_fmaxf(mx, other_half(mx));
        o = zero;
        for (int kt = 0; kt < 64; kt++) {
          f32x16 s = MFMA32(ld16(rsW, l * 16, ks + kt * 4096), q0, zero);
          s = MFMA32(ld16(rsW, l * 16 + 1024, ks + kt * 4096), q1, s);
#pragma unroll
          for (int i = 0; i < 16; i++) s[i] = __builtin_amdgcn_exp2f(s[i] - mx);
          o = MFMA32(ld16(rsW, l * 16 + 2048, ks + kt * 4096), pack8(s, 0), o);
          o = MFMA32(ld16(rsW, l * 16 + 3072, ks + kt * 4096), pack8(s, 8), o);
        }
        lsum = other_half(o[12]);
        lsum = l & 32 ? o[12] : lsum;
      }
    }
    const float r = __builtin_amdgcn_rcpf(lsum), r3 = l & 32 ? 0.f : r;
#pragma unroll
    for (int i = 0; i < 16; i++) o[i] *= i < 12 ? r : r3;
    const unsigned oso = WS_O + ((qb * 4 + w4) * 32 + head * 2) * 1024;
    st16(rsW, __builtin_bit_cast(u32x4, pack8(o, 0)), l * 16, oso);
    st16(rsW, __builtin_bit_cast(u32x4, pack8(o, 8)), l * 16 + 1024, oso);
    drain();
    arrive(lds + L_C3, 4, rsW, WS_F3 + (qb * 16 + head) * 128, tag, l);
  } else {
    if (cb >= 14) tag = phase1<true, 1>(x, wqkv, wg, wu, rsW, rsD, voffD, lds, rb, cb, head, qb, w, l, ep);
    else {
      tag = phase1<false, 1>(x, wqkv, wg, wu, rsW, rsD, voffD, lds, rb, cb, head, qb, w, l, ep);
      __builtin_amdgcn_s_setprio(3);
      geglu_out(ep, rsW, rb, cb, w, l);
      __builtin_amdgcn_s_setprio(PRIO_B);
      drain();
      lds_set(lds + L_W + 32 + w * 4);
      if (w == 4) {
        lds_wait(lds + L_W + 32, 8, l, 1);
        __builtin_amdgcn_raw_buffer_store_b32(tag, rsW, l == 0 ? WS_F2 + (rb * 16 + cb) * 128 : OOB, 0, 16);
      }
    }
    __builtin_amdgcn_s_setprio(PRIO_B);
    if (idx < 28) {
#pragma unroll
    for (int t = 0; t < 4; t++)
#pragma unroll
      for (int i = 0; i < 16; i++) acc[t][i] = 0.f;
    poll(rsW, WS_F2 + ((fr >> 1) * 16 + ((7 * w4) >> 1) + (l & 3)) * 128, tag);
    const unsigned hso = WS_H + (fr * 2 * 112 + w4 * 28) * 1024, ring = lb + w4 * 4096;
    bf16x8 hf[6][4];
    auto ldh = [&](int p) {
#pragma unroll
      for (int i = 0; i < 4; i++) hf[p % 6][i] = ld16(rsW, l * 16 + (i >> 1) * 1024, hso + p * 2048 + ((i & 1) ^ (w4 >> 1)) * (112 * 1024));
    };
#pragma unroll
    for (int p = 0; p < 6; p++) ldh(p);
    SB();
    const int i16 = l & 15;
    const int ab = w4 * 4096 + (4 * (l >> 5) + (i16 >> 2)) * 128 + (((((l >> 4) & 1) * 2 + ((i16 & 3) >> 1)) ^ (((i16 >> 3) ^ (w4 & 1)) << 2)) << 4) + (i16 & 1) * 8;
    const int ad = ab + (fr & 1) * 2048;
    auto pair = [&](int p) {
      bf16x8 wf[4];
#pragma unroll
      for (int i = 0; i < 4; i++) {
        const int o = RS[p % 6] + (i >> 1) * 1024;
        wf[i] = cat(trread(lds + ((ad ^ ((i & 1) << 6)) + o)), trread(lds + ((ad ^ ((i & 1) << 6) ^ 2048) + o)));
      }
#pragma unroll
      for (int i = 0; i < 8; i++) acc[i & 3] = MFMA32(wf[(i >> 2) * 2 + (i & 1)], hf[p % 6][(i >> 2) * 2 + ((i >> 1) & 1)], acc[i & 3]);
      SB();
    };
#pragma unroll
    for (int p = 0; p < 8; p++) {
      if (p >= 6) WAITVM(44);
      pair(p);
      issue_wd(rsD, voffD, p + 6, ring, p % 6);
      ldh(p + 6);
      SB();
    }
    const int tt = fr * 2 + (w4 >> 1), nt = fc * 2 + (w4 & 1);
    const unsigned xo = ((tt * 32 + (l & 31)) * D + nt * 32 + 8 * (l >> 5)) * 2;
    const u32x4 xa = *(const u32x4*)((const char*)x + xo), xb = *(const u32x4*)((const char*)x + xo + 32);
    drain();
    lds_wait(lds + L_W + 16, 4, l, 1);
    const rsrc_t rsO = mkrsrc(wo, D * D * 2);
    const unsigned voffO = voffR + w4 * (112 * 896);
#pragma unroll
    for (int p = 8; p < 14; p++) {
      pair(p);
      if (p < 12) {
#pragma unroll
        for (int i = 0; i < 4; i++) gldsi(rsO, voffO + ((p - 8) * 28 + i * 8) * 896, 0, ring, RS[p % 6] + i * 1024);
      }
    }
    poll(rsW, WS_F3 + ((fr >> 1) * 16 + 4 * w4 + (l & 3)) * 128, tag);
    bf16x8 of[8][2];
#pragma unroll
    for (int s = 0; s < 8; s++)
#pragma unroll
      for (int i = 0; i < 2; i++) of[s][i] = ld16(rsW, l * 16 + (s & 3) * 1024, WS_O + ((fr * 2 + (i ^ (w4 >> 1))) * 32 + w4 * 8 + (s & 4)) * 1024);
    SB();
    bf16x8 wf[8][2];
#pragma unroll
    for (int s = 0; s < 8; s++)
#pragma unroll
      for (int i = 0; i < 2; i++) {
        const int o = RS[2 + (s >> 1)] + (s & 1) * 2048;
        wf[s][i] = cat(trread(lds + ((ab ^ (i << 6)) + o)), trread(lds + ((ab ^ (i << 6)) + o + 1024)));
      }
    SB();
#pragma unroll
    for (int s = 0; s < 8; s++)
#pragma unroll
      for (int i = 0; i < 4; i++) acc[i] = MFMA32(wf[s][i & 1], of[s][i >> 1], acc[i]);
    xwrite(lds, RS, acc, w4, l);
    bar();
    const f32x16 o = xread(lds, RS, acc, w4, l);
#pragma unroll
    for (int a = 0; a < 2; a++) {
      const u32x4 xv = a ? xb : xa;
      float v[8];
#pragma unroll
      for (int i = 0; i < 4; i++) {
        const u32x2 e = __builtin_amdgcn_permlane32_swap(__float_as_uint(o[8 * a + i]), __float_as_uint(o[8 * a + 4 + i]), false, false);
        v[i] = __uint_as_float(e[0]); v[4 + i] = __uint_as_float(e[1]);
      }
      u32x4 r;
#pragma unroll
      for (int i = 0; i < 4; i++) r[i] = pack2(v[2 * i] + lo16(xv[i]), v[2 * i + 1] + hi16(xv[i]));
      *(u32x4*)((char*)out + xo + a * 32) = r;
    }
    } else if (b == 255 && w == 5) {
#pragma unroll
    for (int i = 0; i < 4; i++) poll(rsW, WS_F1 + (i * 64 + l) * 128, tag);
    __builtin_amdgcn_raw_buffer_store_b32(tag, rsW, l == 0 ? WS_EP : OOB, 0, 16);
    }
  }
}

extern "C" void kernel_launch(void* const* d_in, const int* in_sizes, int n_in, void* d_out, int out_size, void* d_ws,
                              size_t ws_size, hipStream_t stream) {
  (void)hipFuncSetAttribute((const void*)fused, hipFuncAttributeMaxDynamicSharedMemorySize, LDS_BYTES);
  fused<<<256, 512, LDS_BYTES, stream>>>((const u16*)d_in[0], (const u16*)d_in[1], (const u16*)d_in[2], (const u16*)d_in[4],
                                         (const u16*)d_in[5], (const u16*)d_in[6], (u16*)d_out, (char*)d_ws);
}
```

```cpp
#include <hip/hip_runtime.h>
#include <hip/hip_bf16.h>
#include <stdint.h>

typedef __attribute__((ext_vector_type(8))) __bf16 bf16x8;
typedef __attribute__((ext_vector_type(2))) __bf16 bf16x2;
typedef __attribute__((ext_vector_type(4))) short s16x4;
typedef __attribute__((ext_vector_type(8))) short s16x8;
typedef __attribute__((ext_vector_type(2))) float f32x2;
typedef __attribute__((ext_vector_type(4))) float f32x4;
typedef __attribute__((ext_vector_type(16))) float f32x16;
typedef __attribute__((ext_vector_type(2))) unsigned u32x2;
typedef __attribute__((ext_vector_type(4))) unsigned u32x4;
typedef __attribute__((ext_vector_type(16))) unsigned u32x16;
typedef __attribute__((ext_vector_type(32))) unsigned u32x32;
typedef __attribute__((address_space(3))) char* ldsp;
typedef unsigned short u16;
typedef __amdgpu_buffer_rsrc_t rsrc_t;
#define DI __device__ __forceinline__
#define LDS(T, a) (*(__attribute__((address_space(3))) T*)(a))

constexpr int S = 2048, D = 448, DF = 1792;
constexpr unsigned WS_Q  = 0;
constexpr unsigned WS_KV = WS_Q + (2u << 20);
constexpr unsigned WS_H  = WS_KV + (4u << 20);
constexpr unsigned WS_O  = WS_H + 64 * 112 * 1024;
constexpr unsigned WS_ST = WS_O + (2u << 20);
constexpr unsigned WS_F1 = WS_ST + 2048 * 16;
constexpr unsigned WS_F2 = WS_F1 + 256 * 128;
constexpr unsigned WS_F3 = WS_F2 + 256 * 128;
constexpr unsigned WS_EP = WS_F3 + 256 * 128;
constexpr unsigned WS_END = WS_EP + 128;
constexpr unsigned OOB = 0xF0000000u;

DI unsigned pack2(float a, float b) {
  f32x2 v = {a, b};
  return __builtin_bit_cast(unsigned, __builtin_convertvector(v, bf16x2));
}
DI unsigned pack2(f32x2 v) { return __builtin_bit_cast(unsigned, __builtin_convertvector(v, bf16x2)); }
DI float lo16(unsigned v) { return __uint_as_float(v << 16); }
DI float hi16(unsigned v) { return __uint_as_float(v & 0xffff0000u); }
DI int rfl(int v) { return __builtin_amdgcn_readfirstlane(v); }
DI rsrc_t mkrsrc(const void* p, unsigned bytes) {
  return __builtin_amdgcn_make_buffer_rsrc((void*)p, (short)0, (int)bytes, 0x00020000);
}
DI void glds16(rsrc_t rs, unsigned voff, unsigned soff, unsigned ldsaddr) {
  asm volatile("s_mov_b32 m0, %0\n\ts_nop 0\n\tbuffer_load_dwordx4 %1, %2, %3 offen lds"
               :: "s"(ldsaddr), "v"(voff), "s"(rs), "s"(soff) : "m0", "memory");
}
DI void st16(rsrc_t rs, u32x4 v, unsigned voff, unsigned soff) {
  asm volatile("buffer_store_dwordx4 %0, %1, %2, %3 offen sc1\n\ts_nop 1" :: "v"(v), "v"(voff), "s"(rs), "s"(soff) : "memory");
}
DI bf16x8 ld16(rsrc_t rs, unsigned voff, unsigned soff) {
  return __builtin_bit_cast(bf16x8, __builtin_amdgcn_raw_buffer_load_b128(rs, voff, soff, 16));
}
DI void drain() { asm volatile("s_waitcnt vmcnt(0)" ::: "memory"); }
DI void arrive(ldsp cnt, int nwaves, rsrc_t rs, unsigned flag, unsigned flag2, unsigned tag, int l) {
  unsigned old;
  asm volatile("ds_add_rtn_u32 %0, %1, %2\n\ts_waitcnt lgkmcnt(0)" : "=v"(old) : "v"((unsigned)(size_t)cnt + l * 4), "v"(1u) : "memory");
  __builtin_amdgcn_raw_buffer_store_b32(tag, rs, l < 2 && old == (unsigned)(nwaves - 1) ? (l ? flag2 : flag) : OOB, 0, 16);
}
constexpr int SPIN_MAX = 1 << 18;
DI void poll(rsrc_t rs, unsigned voff, unsigned tag) {
#pragma unroll 1
  for (int it = 0; it < SPIN_MAX; it++) {
    unsigned v;
    asm volatile("buffer_load_dword %0, %1, %2, 0 offen sc1\n\ts_waitcnt vmcnt(0)" : "=v"(v) : "v"(voff), "s"(rs) : "memory");
    if (__builtin_amdgcn_ballot_w64(v != tag) == 0) break;
    __builtin_amdgcn_s_sleep(2);
  }
}
DI s16x4 trread(ldsp p) {
  return __builtin_amdgcn_ds_read_tr16_b64_v4i16((__attribute__((address_space(3))) s16x4*)p);
}
DI bf16x8 cat(s16x4 a, s16x4 b) {
  s16x8 r = __builtin_shufflevector(a, b, 0, 1, 2, 3, 4, 5, 6, 7);
  return __builtin_bit_cast(bf16x8, r);
}
#define MFMA16(a, b, c) __builtin_amdgcn_mfma_f32_16x16x32_bf16((a), (b), (c), 0, 0, 0)
#define MFMA32(a, b, c) __builtin_amdgcn_mfma_f32_32x32x16_bf16((a), (b), (c), 0, 0, 0)
#define SB() __builtin_amdgcn_sched_barrier(0)
#define WAITVM(n) asm volatile("s_waitcnt vmcnt(%0)" :: "n"(n) : "memory")
DI void bar() {
  __builtin_amdgcn_s_waitcnt(0xC07F);
  asm volatile("s_barrier" ::: "memory");
}

constexpr int XS[4] = {32768, 16384, 0, 49152}, PS = 65536, GB = 98304, GS[4] = {GB + 32768, GB + 49152, GB, GB + 16384};
constexpr int L_CS = PS + 24576, L_ST = L_CS + 2048, L_C1 = L_ST + 2048, L_C3 = L_C1 + 256, L_W = L_C3 + 256, L_J = L_C1 + 1024, LDS_BYTES = 163840;
constexpr int W_CS = L_W, W_AX = L_W + 16, W_H = L_W + 32, W_D = L_W + 64, W_X = L_W + 96, W_S = L_W + 112, W_R = L_W + 128, W_F = L_W + 144;
constexpr int RS[6] = {XS[3], XS[0], PS, GS[2], GS[3], XS[1]}, AX[3] = {XS[2], GS[0], GS[1]};
constexpr int PL[14] = {AX[0], AX[1], AX[2], RS[3], RS[4], RS[5], RS[1], RS[0], RS[2], AX[0], AX[1], RS[3], RS[4], RS[5]};
constexpr int RP[6] = {7, 6, 8, 3, 4, 5}, L_WO = RS[1];
static_assert(AX[0] == 0 && AX[1] == 0x20000 && AX[2] == 0x24000 && RS[5] == 0x4000 && RS[3] == 0x18000 && RS[4] == 0x1c000, "");
static_assert(RS[0] == L_WO + 16384 && RS[2] == L_WO + 32768 && L_WO + D * 128 == L_CS, "");
template <class T> DI T opaque(T v) { asm volatile("" : "+v"(v)); return v; }
#define PIN(v) asm volatile("" : "+v"(v))
#define PIN2(a, b) asm volatile("" : "+v"(a), "+v"(b))

struct Item { int kind, idx, j, need, early; };
struct Plan { Item it[14][2][8]; int n[14][2]; int allow[13][2]; int left; };
#define XO 1
#define PQ2 6
#define TV0 5
constexpr int ST_STEP = 1, SG_STEP = 7;
constexpr int k1_extra(bool isv, int o, int g) {
  if (o == XO) return 2;
  if (o == 4 + ST_STEP) return g ? 1 : 3;
  if (o == 3 + SG_STEP) return g ? 0 : 1;
  if (isv) return o == 3 + TV0 && !g;
  if (o == 3 + 8 || o == 3 + 9) return g ? 0 : 4;
  if (o == 3 + 10) return 4;
  return 0;
}
constexpr Plan k1_plan(bool isv) {
  Item st[100] = {};
  int n = 0;
  for (int b = 1; b <= 12; b++) {
    const int s = b + 1;
    if (!(s & 1) && s > 2 && (isv || s < 10)) for (int j = 0; j < 4; j++) st[n++] = {0, s / 2, j, b, s < 8 ? -9 : s - 7};
    for (int j = 0; j < 4; j++) st[n++] = {1 + (j & 1), s, j >> 1, b < 11 ? b : 11, s < 4 ? -9 : s - 4};
    if (!isv && s < 10) st[n++] = {3, s, 0, b, s < 6 ? -9 : s - 6};
    if (!isv && (b == 8 || b == 9)) {
      for (int j = 0; j < 4; j++) st[n++] = {0, b - 3, j, b, 2 * (b - 3) - 7};
      for (int j = 0; j < 2; j++) st[n++] = {3, 2 * b - 6 + j, 0, b, 2 * b - 12 + j};
    }
  }
  Plan p = {};
  for (int g = 0; g < 2; g++) {
    for (int i = 0; i < 5; i++) p.it[0][g][i].need = i == 4 && (g || isv) ? 0 : -1;
    for (int i = 0; i < 4; i++) p.it[1][g][i].need = i >> 1;
    p.n[0][g] = 5; p.n[1][g] = 4;
  }
  int pos = 0;
  for (int i = 0; i < PQ2 && st[pos].early < 0; i++) p.it[2][i & 1][p.n[2][i & 1]++] = st[pos++];
  for (int s = 0; s < 10; s++)
    for (int g = 1; g >= 0; g--)
      for (int i = 0; i < 4 && pos < n && st[pos].early <= s; i++) p.it[3 + s][g][p.n[3 + s][g]++] = st[pos++];
  p.left = n - pos;
  for (int b = -1; b <= 11; b++)
    for (int g = 0; g < 2; g++) {
      int cnt = 0, last = 0;
      for (int o = 0; o < 14; o++) {
        for (int i = 0; i < p.n[o][g]; i++) {
          if (o <= b + 2) { cnt++; if (p.it[o][g][i].need <= b) last = cnt; }
          else if (p.it[o][g][i].need <= b) p.left = 99;
        }
        if (o <= b + 2) {
          cnt += k1_extra(isv, o, g);
          if ((o == XO && b >= ST_STEP) || (o == 3 + SG_STEP && b >= 9 && !g)) last = cnt;
        }
      }
      p.allow[b + 1][g] = cnt - last;
    }
  return p;
}
constexpr Plan PLAN_R = k1_plan(false), PLAN_V = k1_plan(true);
static_assert(PLAN_R.left == 0 && PLAN_V.left == 0 && PLAN_R.n[12][0] == 0, "stream incomplete");

DI void touch(rsrc_t rs, unsigned voff, unsigned junk) {
  asm volatile("s_mov_b32 m0, %0\n\ts_nop 0\n\tbuffer_load_dword %1, %2, 0 offen lds" :: "s"(junk), "v"(voff), "s"(rs) : "m0", "memory");
}
DI void gldsi(rsrc_t rs, unsigned voff, int soff, unsigned base, int off) {
  unsigned t;
  asm volatile("s_add_u32 m0, %1, %2\n\ts_mov_b32 %0, %3\n\tbuffer_load_dwordx4 %4, %5, %0 offen lds"
               : "=&s"(t) : "s"(base), "i"(off), "i"(soff), "v"(voff), "s"(rs) : "m0", "scc", "memory");
}
DI void gldsr(rsrc_t rs, unsigned voff, unsigned sb, int soff, unsigned base, int off) {
  unsigned t;
  asm volatile("s_add_u32 m0, %1, %2\n\ts_add_u32 %0, %6, %3\n\tbuffer_load_dwordx4 %4, %5, %0 offen lds"
               : "=&s"(t) : "s"(base), "i"(off), "i"(soff), "v"(voff), "s"(rs), "s"(sb) : "m0", "scc", "memory");
}
DI void issue_w(rsrc_t rsD, unsigned voff, int pair, unsigned base, int slot) {
#pragma unroll
  for (int i = 0; i < 4; i++) gldsi(rsD, voff, (pair * 32 + i * 8) * 896, base, slot + i * 1024);
}
DI void issue_wd(rsrc_t rsD, unsigned voff, unsigned base, int i) { issue_w(rsD, voff, RP[i], base, RS[i]); }
#define FL_AT 2
#define BPRIO 0
#define P0 2
#define WSL 1
#define HD 2
#define OROT (fc & 3)
#define UV 1
#define OM ""
#define TW 0
#define TOUT
#define TN 1
#define SGS "12"
#define RSL
#define HS 1
#define XH 0x88772
constexpr unsigned BIG = 0x71800000u;
struct Epi { u32x4 h[4]; unsigned ho, pv; };
DI int lane() {
  int l;
  asm volatile("v_mbcnt_lo_u32_b32 %0, -1, 0\n\tv_mbcnt_hi_u32_b32 %0, -1, %0" : "=v"(l));
  return l;
}
DI unsigned f1_of(int head, int qb, int w4, int l) {
  const int c0 = 7 * (head >> 1) + 3 * (head & 1), e = l < 16 ? l & 3 : 4 + (l & 1);
  const unsigned tab = (c0 >> 3) * 0x10001u + ((c0 + 3) >> 3) * 0x100010u + (head >> 3) * 0x1100u + 0xee77u;
  return WS_F1 + ((l < 16 ? (w4 >> 1) * 8 + (w4 & 1) + (l >> 2) * 2 : qb) * 16 + ((tab >> (4 * e)) & 15)) * 128;
}
DI void lds_wait(ldsp a, int n, int l, int slp) {
#pragma unroll 1
  for (int it = 0; it < SPIN_MAX; it++) {
    unsigned v;
    asm volatile("ds_read_b32 %0, %1\n\ts_waitcnt lgkmcnt(0)" : "=v"(v) : "v"((unsigned)(size_t)a + (l & (n - 1)) * 4) : "memory");
    if (__builtin_amdgcn_ballot_w64(v == 0) == 0) break;
    if (slp == 1) __builtin_amdgcn_s_sleep(1);
    if (slp > 1) __builtin_amdgcn_s_sleep(4);
  }
}
DI void lds_set(ldsp a) { asm volatile("ds_write_b32 %0, %1" :: "v"((unsigned)(size_t)a), "v"(1u) : "memory"); }
DI unsigned voff_x(int rb, int n, int l) { return (rb * 128 + 8 * n + (l >> 3)) * 896 + (((l & 7) ^ ((4 * n + (l >> 4)) & 7)) << 4); }
DI unsigned voff_g(int pitch, int col, int n, int l) { return (4 * n + (l >> 4)) * pitch + col + (((l & 15) ^ (((l >> 4) | ((n & 2) << 1)) << 1)) << 4); }
DI unsigned voff_p(int cb, int n, int l) { return (8 * n + (l >> 3)) * 2688 + cb * 128 + (((l & 7) ^ ((((l >> 4) & 1) | ((n & 1) << 1)) << 1)) << 4); }
DI unsigned voff_r(int fc, int l) { return (l >> 3) * 896 + fc * 128 + (((l & 7) ^ (((l >> 4) & 1) << 2)) << 4); }
DI void red_step(float& v, int i) {
  if (i == 0) asm volatile("s_nop 0\n\tv_add_f32_dpp %0, %0, %0 row_shr:1 row_mask:0xf bank_mask:0xf bound_ctrl:1" : "+v"(v));
  if (i == 1) asm volatile("s_nop 0\n\tv_add_f32_dpp %0, %0, %0 row_shr:2 row_mask:0xf bank_mask:0xf bound_ctrl:1" : "+v"(v));
  if (i == 2) asm volatile("s_nop 0\n\tv_add_f32_dpp %0, %0, %0 row_shr:4 row_mask:0xf bank_mask:0xf bound_ctrl:1" : "+v"(v));
  if (i == 3) asm volatile("s_nop 0\n\tv_add_f32_dpp %0, %0, %0 row_shr:8 row_mask:0xf bank_mask:0xf bound_ctrl:1" : "+v"(v));
  if (i == 4) asm volatile("s_nop 0\n\tv_add_f32_dpp %0, %0, %0 row_bcast:15 row_mask:0xa bank_mask:0xf" : "+v"(v));
  if (i == 5) asm volatile("s_nop 0\n\tv_add_f32_dpp %0, %0, %0 row_bcast:31 row_mask:0xc bank_mask:0xf" : "+v"(v));
}
template <bool PK> DI u32x4 geglu(const f32x4 (&acc)[5][4], const f32x4 (&cs)[4], f32x4 st, int m) {
  const float k1c = -2.0f * 0.7978845608028654f * 1.4426950408889634f * st[2];
  const float k2c = k1c * 0.044715f * st[2] * st[2];
  u32x4 o;
#pragma unroll
  for (int j = 0; j < 4; j++) {
    const int tg = j & 2, i = (j & 1) * 2;
    if (PK) {
      const f32x2 a = (f32x2){acc[tg][m][i], acc[tg][m][i + 1]} - st[0] * (f32x2){cs[tg][i], cs[tg][i + 1]};
      const f32x2 u = (f32x2){acc[tg + 1][m][i], acc[tg + 1][m][i + 1]} - st[0] * (f32x2){cs[tg + 1][i], cs[tg + 1][i + 1]};
      const f32x2 z = a * (k2c * (a * a) + k1c);
      const f32x2 e = {__builtin_amdgcn_exp2f(z[0]), __builtin_amdgcn_exp2f(z[1])};
      const f32x2 dn = e * st[1] + st[1];
      const f32x2 r = {__builtin_amdgcn_rcpf(dn[0]), __builtin_amdgcn_rcpf(dn[1])};
      o[j] = pack2(a * u * r);
    } else {
      float a0, a1, u0, u1, z0, z1;
      asm("v_fma_f32 %0, -%7, %11, %15\n\tv_fma_f32 %1, -%7, %12, %16\n\tv_fma_f32 %2, -%7, %13, %17\n\tv_fma_f32 %3, -%7, %14, %18\n\t"
          "v_mul_f32 %4, %0, %0\n\tv_mul_f32 %5, %1, %1\n\tv_fma_f32 %4, %4, %9, %10\n\tv_fma_f32 %5, %5, %9, %10\n\t"
          "v_mul_f32 %4, %0, %4\n\tv_mul_f32 %5, %1, %5\n\tv_exp_f32 %4, %4\n\tv_exp_f32 %5, %5\n\t"
          "v_mul_f32 %2, %0, %2\n\tv_mul_f32 %3, %1, %3\n\tv_fma_f32 %4, %4, %8, %8\n\tv_fma_f32 %5, %5, %8, %8\n\t"
          "v_rcp_f32 %4, %4\n\tv_rcp_f32 %5, %5\n\tv_mul_f32 %2, %2, %4\n\tv_mul_f32 %3, %3, %5\n\tv_cvt_pk_bf16_f32 %6, %2, %3"
          : "=&v"(a0), "=&v"(a1), "=&v"(u0), "=&v"(u1), "=&v"(z0), "=&v"(z1), "=v"(o[j])
          : "v"(st[0]), "v"(st[1]), "v"(k2c), "v"(k1c), "v"(cs[tg][i]), "v"(cs[tg][i + 1]), "v"(cs[tg + 1][i]), "v"(cs[tg + 1][i + 1]),
            "v"(acc[tg][m][i]), "v"(acc[tg][m][i + 1]), "v"(acc[tg + 1][m][i]), "v"(acc[tg + 1][m][i + 1]));
    }
  }
  return o;
}
#define PK_A 0

template <bool ISV, int mh>
DI unsigned phase1(const u16* x, const u16* wqkv, const u16* wg, const u16* wu, rsrc_t rsW, rsrc_t rsD, ldsp lds, int rb, int cb,
                   int r, int fc, int head, int qb, int w, Epi& ep) {
  const int l = lane(), n = w & 3, g = l >> 4, l15 = l & 15;
  const unsigned lb = (unsigned)(size_t)lds + n * 1024, lr = (unsigned)(size_t)lds + n * 4096;
  constexpr int NT = ISV ? 4 : 5;
  constexpr const Plan& P = ISV ? PLAN_V : PLAN_R;
  const rsrc_t rsX = mkrsrc(x, S * D * 2);
  const int srow = rb * 128 + cb * 8 + w;
  u32x4 xr;
  unsigned tagv;
  asm volatile("buffer_load_dwordx4 %0, %1, %2, %3 offen" : "=v"(xr) : "v"(l < 56 ? l * 16 : OOB), "s"(rsX), "s"(srow * 896) : "memory");
  asm volatile("buffer_load_dword %0, off, %1, %2 sc1" : "=v"(tagv) : "s"(rsW), "s"(WS_EP) : "memory");
  const rsrc_t rsQ = mkrsrc(wqkv, D * 3 * D * 2);
  const rsrc_t rsG = ISV ? rsQ : mkrsrc(wg, D * DF * 2);
  const rsrc_t rsU = ISV ? rsQ : mkrsrc(wu, D * DF * 2);
  constexpr int GP = ISV ? 2688 : 3584;
  const unsigned voffX = voff_x(rb, n, l), voffG = voff_g(GP, ISV ? 1792 + (cb - 14) * 384 : cb * 256, n, l), voffP = voff_p(cb, n, l);
  const unsigned voffD = opaque(voff_r(fc, l) + n * (448 * 896));
  const int gv = (blockIdx.x & 7) * 4 + (rb & 3);
  auto dk = [&](int s) { return (((s & 3) ^ (s < 12 ? r : r & 1)) - (s & 3)) * 32; };
  auto issue1 = [&](int kind, int idx, int j) {
    if (kind == 0) gldsr(rsX, voffX, idx < 6 ? (((idx & 1) ^ (r >> 1)) - (idx & 1)) * 128 : 0, j * 32 * 896 + idx * 128, lb, XS[idx & 3] + j * 4096);
    else if (kind == 1) gldsr(rsG, voffG, dk(idx) * GP, (idx * 32 + j * 16) * GP, lb, GS[idx & 3] + j * 4096);
    else if (kind == 2) gldsr(rsU, voffG, dk(idx) * GP, (idx * 32 + j * 16) * GP + (ISV ? 256 : 0), lb, GS[idx & 3] + 8192 + j * 4096);
    else gldsr(rsQ, voffP, dk(idx) * 2688, idx * (32 * 2688), lb, PS + (idx % 6) * 4096);
  };
#define ISS1(o, i) if constexpr (i < P.n[o][mh]) { constexpr Item t_ = P.it[o][mh][i]; issue1(t_.kind, t_.idx, t_.j); }
#define ISSUE(o) { ISS1(o, 0) ISS1(o, 1) ISS1(o, 2) ISS1(o, 3) }
  unsigned tag = 0;
  LDS(u32x4, lds + L_C1 + l * 16) = (u32x4){0u, 0u, 0u, 0u};
  const int q = l15 >> 2, p = l & 3;
  const int ag = (8 * g + q) * 256 + (((2 * n) ^ (q | ((g & 1) << 2))) << 5) + p * 8;
  ldsp at[4];
#pragma unroll
  for (int t = 0; t < 4; t++) {
    const int pos = ((cb - 14) * 64 + (2 * n + ((t >> 1) ^ mh)) * 56 + (t & 1) * 32 + p * 8) & 511;
    if (ISV) at[t] = opaque(lds + GB + ((pos >> 8) * 8192 + (8 * g + q) * 256 + ((pos & 255) ^ ((q | ((g & 1) << 2)) << 5))));
    else at[t] = (t & 1) ? at[t - 1] + 8192 : opaque(lds + GB + (ag ^ (mh << 5) ^ (t << 4)));
  }
  const ldsp ap = opaque(lds + PS + ((8 * g + q) * 128 + ((n ^ ((q >> 1) | ((g & 1) << 1))) << 5) + p * 8));
  const int ax = ((mh * 64 + l15) * 128 + ((g ^ (l15 >> 1)) << 4)) ^ ((r & 1) << 6);
  const ldsp ax0 = opaque(lds + ax), ax1 = opaque(lds + (ax ^ 64));
  const ldsp cp = opaque(lds + L_CS + (n * 512 + (ISV ? l15 * 4 : g * 16)));
  const ldsp sp = opaque(lds + L_ST + (ISV ? mh * 256 + g * 16 : mh * 1024 + l15 * 16));
  const int tt0 = rb * 4 + mh * 2;
  const unsigned f1 = WS_F1 + (rb * 16 + cb) * 128, f3 = WS_F3 + (qb * 16 + head) * 128;
  const bool isq = cb < 7;
  const int ts = isq ? 2048 : 4096;
  unsigned vo = 0, vo2 = 0;
  if (!ISV) {
    const int c = (isq ? cb : cb - 7) * 8 + 2 * n + (g >> 1), hp = c / 7, c7 = c - hp * 7;
    const int row = isq ? (g & 1) * 16 + l15 : ((l15 & 8) << 1) | ((g & 1) << 3) | (l15 & 7);
    const int sl = c7 & 3;
    vo = (2 * hp + (c7 > 3)) * (64 * ts) + (sl >> 1) * 1024 + (row + 32 * (sl & 1)) * 16;
    vo2 = c7 == 3 ? vo + 64 * ts : OOB;
    asm volatile("" : "+v"(vo), "+v"(vo2));
  }

  f32x4 acc[5][4], cs[5];
#pragma unroll
  for (int t = 0; t < 5; t++) {
    cs[t] = (f32x4){0.f, 0.f, 0.f, 0.f};
#pragma unroll
    for (int m = 0; m < 4; m++) acc[t][m] = (f32x4){0.f, 0.f, 0.f, 0.f};
  }
  bf16x8 ones = __builtin_bit_cast(bf16x8, (u32x4){0x3f803f80u, 0x3f803f80u, 0x3f803f80u, 0x3f803f80u});
  PIN(ones);
  s16x4 wl[2][5], wh[2][5], el[2], eh[2]; bf16x8 xf[2][4], xe[2][4];
  auto rdx = [&](int s, int r) { return LDS(const bf16x8, ((s & 1) ? ax1 : ax0) + (XS[(s >> 1) & 3] + r * 2048)); };
  auto rd = [&](int s, int r) {
    const int bs = s & 1;
    if (r < 4) xf[bs][r] = rdx(s, r);
    else {
      const int t = (r - 4) >> 1, hi = (r - 4) & 1;
      if (t < 4 || (t < NT && s < 10)) {
        const ldsp a = t == 4 ? ap + (hi * 512 + (s % 6) * 4096) : at[t & 3] + (hi * 1024 + GS[s & 3] - GB);
        if (hi) wh[bs][t] = trread(a); else wl[bs][t] = trread(a);
      }
    }
  };
  auto rde = [&](int s, int r) {
    const int e = r / 6, k = 2 * s - 6 + e, i = r % 6;
    if (i < 4) xe[e][i] = rdx(k, i);
    else if (i == 4) el[e] = trread(ap + (k % 6) * 4096);
    else eh[e] = trread(ap + (512 + (k % 6) * 4096));
  };
  auto mm = [&](int s, int t, int m) {
    const bf16x8 wf = cat(wl[s & 1][t], wh[s & 1][t]);
    acc[t][m] = ISV ? MFMA16(xf[s & 1][m], wf, acc[t][m]) : MFMA16(wf, xf[s & 1][m], acc[t][m]);
  };
  auto mc = [&](int s, int t) {
    const bf16x8 wf = cat(wl[s & 1][t], wh[s & 1][t]);
    cs[t] = ISV ? MFMA16(ones, wf, cs[t]) : MFMA16(wf, ones, cs[t]);
  };
  float sv[8], sa, sb, mean;
  u32x4 so;
  auto stats = [&](int c) {
    if (c == 0) { asm volatile("" : "+v"(xr), "+v"(tagv)); tag = rfl(tagv) + 1; }
    if (c < 4) { sv[2 * c] = lo16(xr[c]); sv[2 * c + 1] = hi16(xr[c]); PIN2(sv[2 * c], sv[2 * c + 1]); }
    else if (c == 4) { sa = sv[0] + sv[1]; sb = sv[2] + sv[3]; PIN2(sa, sb); }
    else if (c < 7) { sa += sv[2 * c - 6]; sb += sv[2 * c - 5]; PIN2(sa, sb); }
    else if (c == 7) { sa += sb; PIN(sa); }
    else if (c < 14) red_step(sa, c - 8);
    else if (c == 14) { sa *= 1.0f / D; PIN(sa); }
    else if (c == 15) { mean = __int_as_float(__builtin_amdgcn_readlane(__float_as_int(sa), 63)); asm volatile("" : "+s"(mean)); }
    else if (c < 20) { sv[2 * c - 32] -= mean; sv[2 * c - 31] -= mean; PIN2(sv[2 * c - 32], sv[2 * c - 31]); }
    else if (c == 20) { sa = sv[0] * sv[0]; sb = sv[1] * sv[1]; PIN2(sa, sb); }
    else if (c < 24) { sa += sv[2 * c - 40] * sv[2 * c - 40]; sb += sv[2 * c - 39] * sv[2 * c - 39]; PIN2(sa, sb); }
    else if (c == 24) { sa += sb; sa = l < 56 ? sa : 0.f; PIN(sa); }
    else if (c < 31) red_step(sa, c - 25);
    else if (c == 31) { so = (u32x4){__float_as_uint(mean), tag, __float_as_uint(sa * (1.0f / D) + 1e-5f), tag}; PIN(so); }
  };
  u32x4 sg;
  const unsigned voffS = WS_ST + (rb * 128 + n * 32 + (l & 31)) * 16;
  auto pin = [&]() {
#pragma unroll
    for (int t = 0; t < 4; t++) asm volatile("" : "+v"(acc[t][0]), "+v"(acc[t][1]), "+v"(acc[t][2]), "+v"(acc[t][3]));
  };
  auto stats_in = [&]() {
    pin();
    asm volatile("" : "+v"(sg), "+v"(cs[0]), "+v"(cs[1]));
    if (!ISV) asm volatile("" : "+v"(acc[4][0]), "+v"(acc[4][1]), "+v"(acc[4][2]), "+v"(acc[4][3]), "+v"(cs[4]));
#pragma unroll 1
    for (int it = 0; it < SPIN_MAX && __builtin_amdgcn_ballot_w64(sg[1] != tag || sg[3] != tag) != 0; it++) {
      __builtin_amdgcn_s_sleep(1);
      asm volatile("buffer_load_dwordx4 %0, %1, %2, 0 offen sc1\n\ts_waitcnt vmcnt(0)" : "=v"(sg) : "v"(voffS), "s"(rsW) : "memory");
    }
    const float rs = __builtin_amdgcn_rsqf(__uint_as_float(sg[2]));
    if (ISV) {
      const ldsp a = lds + L_ST + (n * 32 + (l & 31)) * 4;
      LDS(unsigned, a) = sg[0]; LDS(float, a + 512) = rs;
    } else {
      const float sc = isq ? 0.18898223650461362f * 1.4426950408889634f : 1.0f;
      LDS(f32x4, lds + L_ST + (n * 32 + (l & 31)) * 16) = (f32x4){__uint_as_float(sg[0]), __uint_as_float(sg[2]), rs, rs * sc};
    }
  };
  f32x4 st[4], rs[4], cv;
  float cq[4], qv[2][4];
  unsigned qp[4];
  auto qk_out = [&](int c) {
#define PIN4(a) asm volatile("" : "+v"(a[0]), "+v"(a[1]), "+v"(a[2]), "+v"(a[3]))
    const int a = c >= 10, k = c - 4 - 6 * a, m = 2 * a + (k >> 1);
    if (c == 0) cv = LDS(f32x4, cp + (1 - mh) * 64);
    else if (c == 1) {
#pragma unroll
      for (int i = 0; i < 4; i++) st[i] = LDS(f32x4, sp + i * 256);
    } else if (c == 3) {
#pragma unroll
      for (int i = 0; i < 4; i++) cq[i] = cv[i] + cs[4][i];
      PIN4(cq);
    } else if (c >= 4 && k < 4) {
#pragma unroll
      for (int i = 0; i < 4; i++) qv[m & 1][i] = (k & 1) ? qv[m & 1][i] * st[m][3] : __builtin_fmaf(-st[m][0], cq[i], acc[4][m][i]);
      PIN4(qv[m & 1]);
    } else if (k == 4) {
#pragma unroll
      for (int i = 0; i < 4; i++) qp[i] = pack2(qv[i & 1][i & 2], qv[i & 1][(i & 2) + 1]);
      PIN4(qp);
    } else if (k == 5) {
      const u32x2 e0 = __builtin_amdgcn_permlane16_swap(qp[0], qp[1], false, false), e1 = __builtin_amdgcn_permlane16_swap(qp[2], qp[3], false, false);
      const bool sh = vo2 != OOB;
      const unsigned so = (isq ? WS_Q : WS_KV) + (tt0 + a) * ts;
      u32x4 o = {e0[0], e1[0], sh ? 0u : e0[1], sh ? 0u : e1[1]}, o2 = {0u, 0u, e0[1], e1[1]};
      if (!mh && !a) WAITVM(8);
      st16(rsW, o, vo, so);
      asm volatile("s_cmp_eq_u64 %4, 0\n\ts_cbranch_scc1 1f\n\tbuffer_store_dwordx4 %0, %1, %2, %3 offen sc1\n\ts_nop 1\n1:"
                   :: "v"(o2), "v"(vo2), "s"(rsW), "s"(so), "s"(__builtin_amdgcn_ballot_w64(sh)) : "scc", "memory");
    }
  };
  auto cs_wr = [&](int t, int o) { if (ISV) LDS(float, cp + o) = cs[t][0]; else LDS(f32x4, cp + o) = cs[t]; };
  auto cs_rd = [&](int o) { return ISV ? (f32x4){LDS(float, cp + o), 0.f, 0.f, 0.f} : LDS(f32x4, cp + o); };
  WAITVM(P.allow[0][mh]);
  bar();
#pragma unroll
  for (int r = 0; r < 14; r++) rd(0, r);
  ISSUE(2)
  WAITVM(P.allow[1][mh]);
#define K1_STEP(s)                                                                          \
  {                                                                                         \
    bar();                                                                                  \
    constexpr bool ext = !ISV && (s == 8 || s == 9);                                        \
    constexpr int nm = (ISV || s >= 10) ? 16 : ext ? 28 : 20;                               \
    if (mh) ISSUE(3 + s)                                                                    \
    if (!ISV && mh && s == 10) issue_wd(rsD, voffD, lr, 2);                                 \
    SB();                                                                                   \
    if (!ISV && s < 10 && (s & 1) == mh) mc(s, 4);                                          \
    _Pragma("unroll") for (int i = 0; i < nm; i++) {                                        \
      if (i < 20) mm(s, i >> 2, i & 3);                                                     \
      else acc[4][i & 3] = MFMA16(cat(el[(i >> 2) - 5], eh[(i >> 2) - 5]), xe[(i >> 2) - 5][i & 3], acc[4][i & 3]); \
      if (!ISV && s == 10 && i < 2) qk_out(i);                                              \
      if (ext) { if (i < 12) rde(s, i); else if (i < 26) rd(s + 1, i - 12); }               \
      else if (i < 14) rd(s + 1, i);                                                        \
      SB();                                                                                 \
      if (!ISV && s == 10 && i >= 2 && !mh) { qk_out(i); SB(); }                            \
      if ((s == ST_STEP || s == ST_STEP + 1) && i < 16) { stats((s - ST_STEP) * 16 + i); SB(); } \
      if (i == 3 || i == 7) { mc(s, i >> 2); SB(); }                                        \
      if (ext && i == 16) { cs[4] = MFMA16(cat(el[mh], eh[mh]), ones, cs[4]); SB(); }       \
      if (!ISV && s == 9 && i == 20) LDS(f32x4, cp + mh * 64) = cs[4];                      \
      if (s == 9 && i == 8 && !mh) stats_in();                                              \
      if (s == 10 && i == 10 && mh) { cs_wr(0, 128); cs_wr(1, 192); }                       \
    }                                                                                       \
    if (!mh) ISSUE(3 + s)                                                                   \
    if (!ISV && !mh && s >= 8) issue_wd(rsD, voffD, lr, s - 8 + s / 10);                    \
    if (s == ST_STEP + 1) st16(rsW, so, l == 63 ? 0 : OOB, WS_ST + srow * 16);              \
    if (s == ST_STEP + 1 && !mh) {                                                          \
      st16(rsW, (u32x4){~0u, ~0u, ~0u, ~0u}, l * 16, WS_O + ((qb * 4 + n) * 32 + head * 2) * 1024);        \
      st16(rsW, (u32x4){~0u, ~0u, ~0u, ~0u}, l * 16, WS_O + ((qb * 4 + n) * 32 + head * 2 + 1) * 1024);    \
    }                                                                                       \
      \
    if (ISV && s == TV0 && !mh) touch(mkrsrc(wg, DF * D * 2), l < 63 ? (gv >> 3) * 401408 + ((gv & 7) * 252 + n * 63 + l) * 128 : OOB, (unsigned)(size_t)lds + L_J); \
    if (s == SG_STEP && !mh) asm volatile("buffer_load_dwordx4 %0, %1, %2, 0 offen sc1" : "=v"(sg) : "v"(voffS), "s"(rsW) : "memory"); \
    if (ISV || mh || s < 10) WAITVM(P.allow[s + 2][mh]);                                    \
    SB();                                                                                   \
  }
  K1_STEP(0) K1_STEP(1) K1_STEP(2) K1_STEP(3) K1_STEP(4) K1_STEP(5) K1_STEP(6) K1_STEP(7) K1_STEP(8) K1_STEP(9) K1_STEP(10)
  bar();
  if (!mh) __builtin_amdgcn_s_setprio(3);
  if (mh && !ISV) {
#pragma unroll
    for (int c = 2; c < 16; c++) qk_out(c);
    issue_wd(rsD, voffD, lr, 4);
    issue_wd(rsD, voffD, lr, 5);
    WAITVM(8);
    arrive(lds + L_C1, 8, rsW, f1, f3, tag, l);
    SB();
  }
  f32x4 cb0, cb1;
#pragma unroll
  for (int s = 11; s < 13; s++) {
#pragma unroll
    for (int i = 0; i < 16; i++) {
      mm(s, i >> 2, i & 3);
      if (i < 12) rd(s + 1, i);
      else if (ISV && s == 11) st[i - 12] = LDS(f32x4, sp + (i - 12) * 64);
      else if (ISV) rs[i - 12] = LDS(f32x4, sp + 512 + (i - 12) * 64);
      if (!mh && s == 12 && i == 12) { cb0 = cs_rd(128); cb1 = cs_rd(192); }
      SB();
      if (!mh && (i & 3) == 3) { mc(s, i >> 2); SB(); }
    }
  }
  if (!mh) {
#pragma unroll
    for (int t = 3; t >= 0; t--) mc(13, t);
    SB();
  }
#pragma unroll
  for (int i = 0; i < 16; i++) {
    mm(13, i >> 2, i & 3);
    if (!mh && i == 1) {
#pragma unroll
      for (int j = 0; j < (ISV ? 1 : 4); j++) { cs[3][j] += cb1[j]; PIN(cs[3]); }
      cs_wr(3, 320);
    }
    if (!mh && i == 2) {
#pragma unroll
      for (int j = 0; j < (ISV ? 1 : 4); j++) { cs[2][j] += cb0[j]; PIN(cs[2]); }
      cs_wr(2, 256);
    }
    if (!mh && i == 3) { cs_wr(0, 384); cs_wr(1, 448); lds_set(lds + W_CS + n * 4); }
    SB();
  }
  pin();
  if (mh) {
    lds_set(lds + W_D + 16 + n * 4);
    lds_wait(lds + W_CS + n * 4, 1, l, 0);
#pragma unroll
    for (int t = 0; t < 4; t++) cs[t] = cs_rd(256 + t * 64);
    __builtin_amdgcn_s_setprio(2);
  } else if (!ISV) {
    WAITVM(4);
    arrive(lds + L_C1, 8, rsW, f1, f3, tag, l);
  }
  const unsigned ho = (g >> 1) * 1024 + (l15 + 32 * (g & 1)) * 16;
  if (ISV) {
    const unsigned keep = l15 < 12 ? ~0u : 0u, pad = l15 == 12 ? 0x3f803f80u : 0u;
#pragma unroll
    for (int t = 0; t < 4; t++)
#pragma unroll
      for (int a = 0; a < 2; a++) {
        const f32x4 v0 = (acc[t][2 * a] - st[2 * a] * cs[t][0]) * rs[2 * a], v1 = (acc[t][2 * a + 1] - st[2 * a + 1] * cs[t][0]) * rs[2 * a + 1];
        u32x4 o = {pack2(v0[0], v0[1]), pack2(v0[2], v0[3]), pack2(v1[0], v1[1]), pack2(v1[2], v1[3])};
        if (t & 1) o = (o & keep) | pad;
        st16(rsW, o, ho, WS_KV + 2048 + (t & 1) * 256 + (((cb - 14) * 8 + 2 * n + ((t >> 1) ^ mh)) * 64 + tt0 + a) * 4096);
      }
    drain();
    arrive(lds + L_C1, 8, rsW, f1, f3, tag, l);
    ep.pv = tag - 1;
    ep.ho = OOB;
#pragma unroll
    for (int m = 0; m < 4; m++) asm volatile("" : "=v"(ep.h[m]));
    if (mh) {
#pragma unroll
      for (int i = 0; i < 6; i++) issue_wd(rsD, voffD, lr, i);
    } else lds_set(lds + W_D + n * 4);
  } else {
    const f32x4 c4[4] = {cs[0], cs[1], cs[2], cs[3]};
    const unsigned hso = WS_H + (tt0 * 112 + cb * 8 + n * 2) * 1024;
#pragma unroll
    for (int m = 0; m < 4; m++) {
      if (!mh && m == FL_AT) asm volatile("buffer_load_dword %0, %1, %2, 0 offen sc1" : "=v"(ep.pv) : "v"(f1_of(head, qb, n, l)), "s"(rsW) : "memory");
      ep.h[m] = geglu<mh || PK_A>(acc, c4, st[m], m);
      PIN(ep.h[m]);
      SB();
    }
    ep.ho = ho + hso;
    if (!mh) {
      asm volatile("s_waitcnt vmcnt(0)" : "+v"(ep.pv) :: "memory");
      lds_set(lds + W_D + n * 4);
    }
  }
  return tag;
}

DI bf16x8 pack8(const f32x16& s, int o) {
  u32x4 r = {pack2(s[o], s[o + 1]), pack2(s[o + 2], s[o + 3]), pack2(s[o + 4], s[o + 5]), pack2(s[o + 6], s[o + 7])};
  return __builtin_bit_cast(bf16x8, r);
}
DI float other_half(float v) {
  const u32x2 e = __builtin_amdgcn_permlane32_swap(__float_as_uint(v), __float_as_uint(v), false, false);
  return __uint_as_float(lane() & 32 ? e[0] : e[1]);
}

DI int sel(bool c, int a, int b) {
  asm volatile("" : "+s"(a), "+s"(b));
  return c ? a : b;
}
typedef __attribute__((ext_vector_type(8))) unsigned long long u64x8;

__global__ __launch_bounds__(512, 2) void fused(const u16*, const u16*, const u16*, const u16*, const u16*, const u16*, u16*, char*) {
  extern __shared__ __attribute__((aligned(16))) char smem[];
  u64x8 ka;
  asm volatile("s_load_dwordx16 %0, %1, 0x0" : "=s"(ka) : "s"(__builtin_amdgcn_kernarg_segment_ptr()));
  const ldsp lds = (ldsp)smem;
  const unsigned lb = (unsigned)(size_t)lds;
  const int b = blockIdx.x, w = rfl(threadIdx.x >> 6), w4 = w & 3;
  const int xcd = b & 7, idx = b >> 3;
  {
    unsigned long long pc;
    unsigned len;
    asm volatile("s_getpc_b64 %0\n.Lcode_beg:\n\ts_mov_b32 %1, .Lcode_end-.Lcode_beg" : "=s"(pc), "=s"(len));
    asm volatile("s_mov_b32 m0, %0\n\ts_nop 0\n\tbuffer_load_dword %1, %2, 0 offen lds"
                 :: "s"(lb + L_J), "v"(((idx & 7) * 64 + threadIdx.x) * 128), "s"(mkrsrc((const void*)pc, w ? 0 : len)) : "m0", "memory");
  }
  const int rb = (xcd >> 2) * 8 + (idx >> 2);
  const int cb = sel((idx & 3) < 3, (xcd & 3) * 3 + (idx & 3), (((idx >> 4) ^ xcd) & 1) * 2 + 12 + ((xcd >> 1) & 1));
#define ROT (((idx >> 2) & 1) | ((xcd >> 2) << 1))
  const int r = ROT;
  {
    const int l = threadIdx.x & 63, mh = w >> 2;
    const bool isv = cb >= 14;
    const int gp = isv ? 2688 : 3584;
    unsigned vx = voff_x(rb, w4, l) + mh * (32 * 896), vg = voff_g(gp, sel(isv, 1792 + (cb - 14) * 384 + mh * 256, cb * 256), w4, l);
    unsigned vp = voff_p(cb, w4, l) | (isv ? OOB : 0u);
    unsigned bx = lb + w4 * 1024 + mh * 4096, bg = bx + mh * 4096, sx = (r >> 1) * 128, sg = r * 32 * gp, sg1 = (r ^ 1) * 32 * gp;
    asm volatile("s_waitcnt lgkmcnt(0)" : "+s"(ka), "+v"(vx), "+v"(vg), "+v"(vp), "+s"(bx), "+s"(bg), "+s"(sx), "+s"(sg), "+s"(sg1));
    const rsrc_t rsX = mkrsrc((void*)ka[0], S * D * 2), rsQ = mkrsrc((void*)ka[1], D * 3 * D * 2);
    const rsrc_t rsG = mkrsrc((void*)(isv ? ka[1] : mh ? ka[4] : ka[3]), D * gp);
    gldsr(rsX, vx, sx, 0, bx, XS[0]);
    gldsr(rsX, vx, sx, 64 * 896, bx, XS[0] + 8192);
    gldsr(rsG, vg, sg, 0, bg, GS[0]);
    gldsr(rsG, vg, sg + 16 * gp, 0, bg, GS[0] + 4096);
    gldsr(rsQ, vp, (r ^ mh) * (32 * 2688), 0, bx, PS);
    asm volatile("s_barrier" ::: "memory");
    gldsr(rsG, vg, sg1, 0, bg, GS[1]);
    gldsr(rsG, vg, sg1 + 16 * gp, 0, bg, GS[1] + 4096);
    gldsr(rsX, vx, sx ^ 128, 0, bx, XS[1]);
    gldsr(rsX, vx, sx ^ 128, 64 * 896, bx, XS[1] + 8192);
  }
  const u16 *x = (const u16*)ka[0], *wqkv = (const u16*)ka[1], *wo = (const u16*)ka[2], *wg = (const u16*)ka[3], *wu = (const u16*)ka[4], *wd = (const u16*)ka[5];
  char* ws = (char*)ka[7];
  const int head = xcd * 2 + ((idx >> 1) & 1), qb = (idx >> 2) + (idx & 1) * 8;
  const int fr = xcd * 4 + idx / 7, fc = idx % 7;
  const rsrc_t rsW = mkrsrc(ws, WS_END), rsD = mkrsrc(wd, idx < 28 ? DF * D * 2 : 0);
  unsigned tag;
  Epi ep;
  f32x16 acc[4];
  if (w < 4) {
    if (cb >= 14) tag = phase1<true, 0>(x, wqkv, wd, wo, rsW, rsD, lds, rb, cb, r, fc, head, qb, w, ep);
    else tag = phase1<false, 0>(x, wqkv, wg, wu, rsW, rsD, lds, rb, cb, r, fc, head, qb, w, ep);
    const int l = lane();
    const unsigned voffF = f1_of(head, qb, w4, l);
    unsigned pv = ep.pv;
#pragma unroll 1
    for (int it = 0; it < SPIN_MAX && (__builtin_amdgcn_ballot_w64(pv != tag) & ~0xfff0ull) != 0; it++) {
      __builtin_amdgcn_s_sleep(1);
      asm volatile("buffer_load_dword %0, %1, %2, 0 offen sc1\n\ts_waitcnt vmcnt(0)" : "=v"(pv) : "v"(voffF), "s"(rsW) : "memory");
    }
    const unsigned qso = WS_Q + (head * 64 + qb * 4) * 2048, kso = WS_KV + (head * 64 + (w4 >> 1) * 32 + (w4 & 1) * 4) * 4096;
    const unsigned vl = l * 16;
    const unsigned oso = WS_O + ((qb * 4 + w4) * 32 + head * 2) * 1024;
    unsigned bad;
    {
      u32x4 rsk = {(unsigned)(size_t)ws, (unsigned)((size_t)ws >> 32) & 0xffffu, WS_END, 0x00020000u};
      unsigned ko = kso;
#define V10(a) "v" #a "0", "v" #a "1", "v" #a "2", "v" #a "3", "v" #a "4", "v" #a "5", "v" #a "6", "v" #a "7", "v" #a "8", "v" #a "9"
#define R4(b) "v[" #b ":" #b "+3]"
#define R16(b) "v[" #b ":" #b "+15]"
#define MF(d, a, b, c) "v_mfma_f32_32x32x16_bf16 " R16(d) ", " R4(a) ", " R4(b) ", " c "\n\t"
#define EX(d, s) "v_exp_f32_e32 v[" #d "], v[" #s "]\n\t"
#define EXL(d, s) "v_exp_f32_e64 v[" #d "], v[" #s "]\n\t"
#define CV(d, a) "v_cvt_pk_bf16_f32 v[" #d "], v[" #a "], v[" #a "+1]\n\t"
#define LDV(r, v, s, o) "buffer_load_dwordx4 " R4(r) ", v" #v ", s[64:67], " s " offen offset:" #o " sc1\n\t"
#define DSW(a, r, o) "ds_write_b128 v" #a ", " R4(r) " offset:" #o "\n\t"
#define DSR(r, a, o) "ds_read_b128 " R4(r) ", v" #a " offset:" #o "\n\t"
#define AD1(d, s) "v_add_f32 v[" #d "], v[" #d "], v[" #s "]\n\t"
#define AD4(d, s) AD1(d, s) AD1(d+1, s+1) AD1(d+2, s+2) AD1(d+3, s+3)
#define AD12(d, s) AD4(d, s) AD4(d, s+4) AD4(d, s+8)
#define G_EX(d, s) EXL(d, s)
#define G_EXL(d, s) EX(d, s)
#define UNIT(E0, SC, SN, KN, QN, AP, VP, H1, H2, H3, H4)                                                    \
  MF(SN, KN, QN, "0") H1 EX(SC, SC) EX(SC+1, SC+1) G_##E0(SC+2, SC+2)                                       \
  MF(AP, VP, 52, R16(AP)) H2 EX(SC+3, SC+3) EX(SC+4, SC+4) EXL(SC+5, SC+5)                                  \
  MF(SN, KN+4, QN+4, R16(SN)) H3 EX(SC+6, SC+6) EX(SC+7, SC+7) EXL(SC+8, SC+8)                              \
  MF(AP, VP+4, 56, R16(AP)) H4 CV(52, SC) CV(53, SC+2) CV(54, SC+4) CV(55, SC+6)                            \
  EX(SC+9, SC+9) EX(SC+10, SC+10) EX(SC+11, SC+11) EX(SC+12, SC+12) EX(SC+13, SC+13) EX(SC+14, SC+14) EXL(SC+15, SC+15) \
  CV(56, SC+8) CV(57, SC+10) CV(58, SC+12) CV(59, SC+14)
#define TILE(K, KN, KP, LV, E0, E12, PRE1, PRE2, PRE3, POST3)                                               \
  UNIT(E0, 64, 80, K, 232, 144, KP+8, "", "", "", LDV(KP, LV, "s68", 0)) PRE1                               \
  UNIT(E12, 80, 64, K, 240, 96, K+8, "", "", "", LDV(KP+4, LV, "s68", 1024)) PRE2                           \
  UNIT(E12, 64, 80, K, 248, 112, K+8, "", "", "", LDV(KP+8, LV, "s68", 2048))                               \
  "s_waitcnt vmcnt(7)\n\t" PRE3                                                                             \
  UNIT(EXL, 80, 64, KN, 224, 128, K+8, "", "", "", LDV(KP+12, LV, "s68", 3072) POST3)
#define STH(r, s, o) "buffer_store_dwordx4 " r ", %12, s[64:67], " s " offen offset:" #o " sc1\n\t"
#define SPIN(L, O, BACK)                                                                                    \
  #L ":\n\tds_read_b32 v45, v44 offset:" #O "\n\ts_waitcnt lgkmcnt(0)\n\tv_cmp_eq_u32 vcc, 0, v45\n\t"      \
  "s_cbranch_vccz " BACK "\n\ts_sub_u32 s71, s71, 1\n\ts_cbranch_scc0 " #L "b\n\ts_mov_b32 s71, 0\n\ts_branch 8f\n"
#define MX(a) "v_maximum3_f32 v64, |v[" #a "]|, |v[" #a "+1]|, v64\n\tv_maximum3_f32 v65, |v[" #a "+2]|, |v[" #a "+3]|, v65\n\t"
#define ML1(d, r) "v_mul_f32 v[" #d "], v[" #d "], v" #r "\n\t"
#define NRM(a, r, p) MX(a) ML1(a, r) ML1(a+1, 62) ML1(a+2, 62) ML1(a+3, 62) CV(p, a) CV(p+1, a+2)
      asm volatile(
          LDV(160, 48, "s68", 0) LDV(164, 48, "s68", 1024) LDV(224, 48, "%4", 0) LDV(228, 48, "%4", 1024)
          LDV(232, 48, "%5", 0) LDV(236, 48, "%5", 1024) LDV(168, 48, "s68", 2048) LDV(240, 48, "%6", 0)
          LDV(244, 48, "%6", 1024) LDV(172, 48, "s68", 3072) LDV(248, 48, "%7", 0) LDV(252, 48, "%7", 1024)
          "v_add_u32 v49, 0x1000, v48\n\tv_add_u32 v50, 0x2000, v48\n\tv_add_u32 v51, 0x3000, v48\n\ts_mov_b32 s69, 0x1c000\n\t"
          LDV(176, 49, "s68", 0) LDV(180, 49, "s68", 1024) LDV(184, 49, "s68", 2048) LDV(188, 49, "s68", 3072)
          STH("%8", "0", 0) STH("%9", "0", 256) STH("%10", "s69", 0) STH("%11", "s69", 256)
          LDV(192, 50, "s68", 0) LDV(196, 50, "s68", 1024) LDV(200, 50, "s68", 2048) LDV(204, 50, "s68", 3072)
          "s_setprio 1\n\t"
          "v_mov_b32 v220, 0\n\tv_mov_b32 v221, 0\n\tv_mov_b32 v222, 0\n\tv_mov_b32 v223, 0\n\t"
          "v_mov_b32 v216, 0\n\tv_mov_b32 v217, 0\n\tv_mov_b32 v218, 0\n\tv_mov_b32 v219, 0\n\t"
          "v_mov_b32 v52, 0\n\tv_mov_b32 v53, 0\n\tv_mov_b32 v54, 0\n\tv_mov_b32 v55, 0\n\t"
          "v_mov_b32 v56, 0\n\tv_mov_b32 v57, 0\n\tv_mov_b32 v58, 0\n\tv_mov_b32 v59, 0\n\t"
          MF(96, 220, 220, "0") MF(112, 220, 220, "0") MF(128, 220, 220, "0") MF(144, 220, 220, "0")
          "v_add_u32 v39, 0x20000, v43\n\tv_xor_b32 v40, 0x3000, v43\n\tv_xor_b32 v41, 0x2000, v39\n\tv_xor_b32 v42, 0x1000, v39\n\t"
          "s_add_u32 s69, s68, 0x20000\n\ts_mov_b64 s[72:73], 0xf0\n\ts_mov_b32 s71, 0x40000\n\t"
          "s_waitcnt vmcnt(20)\n\t"
          MF(64, 160, 224, "0") MF(64, 164, 228, R16(64))
          "s_waitcnt vmcnt(17)\n\ts_nop 10\n\t"
          ".p2align 3\n"
          "1:\n\t"
          TILE(160, 176, 208, 51, EX, EXL, "s_waitcnt vmcnt(15)\n\t", "s_waitcnt vmcnt(14)\n\t", "ds_write_b32 v47, v47 offset:32\n\t",
               "v_cmp_ne_u32 vcc, s70, v45\n\ts_and_b64 vcc, vcc, s[72:73]\n\ts_cbranch_scc1 2f\n"
               "3:\n\ts_lshl_b64 s[72:73], s[72:73], 4\n\t"
               "s_add_u32 s68, s68, 0x8000\n\ts_cmp_lg_u32 s68, s69\n\ts_cselect_b32 s66, s66, 0\n\t")
          TILE(176, 192, 160, 48, EX, EX, "", "", "", "")
          TILE(192, 208, 176, 49, EX, EX, "", "", "", "buffer_load_dword v45, v46, s[64:67], 0 offen sc1\n\t")
          "s_cbranch_scc0 4f\n\t"
          TILE(208, 160, 192, 50, EXL, EX, "", "", "", "")
          "s_branch 1b\n"
          "2:\n\ts_sleep 1\n\tbuffer_load_dword v45, v46, s[64:67], 0 offen sc1\n\ts_waitcnt vmcnt(0)\n\t"
          "s_cmp_eq_u32 s71, 0\n\ts_cbranch_scc1 3b\n\ts_sub_u32 s71, s71, 1\n\t"
          "v_cmp_ne_u32 vcc, s70, v45\n\ts_and_b64 vcc, vcc, s[72:73]\n\ts_cbranch_scc1 2b\n\ts_branch 3b\n"
          SPIN(6, 96, "5f") SPIN(9, 16, "7f")
          ".p2align 3\n"
          "4:\n\t"
          UNIT(EX, 64, 80, 208, 232, 144, 200, "", "", "", "s_waitcnt vmcnt(0)\n\ts_mov_b32 s72, -1\n\t")
          UNIT(EX, 80, 64, 208, 240, 96, 216, "ds_read_b32 v45, v44 offset:96\n\t", "s_mov_b32 s66, %17\n\t", "", "")
          "s_waitcnt lgkmcnt(0)\n\tv_cmp_eq_u32 vcc, 0, v45\n\ts_mov_b32 s73, 0\n\ts_cbranch_vccnz 6b\n"
          "5:\n\t"
          UNIT(EX, 64, 80, 208, 248, 112, 216, DSW(43, 96, 0), DSW(43, 100, 1024), DSW(43, 104, 2048), DSW(43, 108, 3072))
          MF(128, 216, 52, R16(128)) EX(80, 80) EX(81, 81) DSW(39, 112, 0) EX(82, 82) EX(83, 83) MF(128, 220, 56, R16(128))
          CV(52, 80) DSW(39, 116, 1024) EX(84, 84) EX(85, 85) CV(53, 82)
          DSW(39, 120, 2048) EX(86, 86) EX(87, 87) CV(54, 84) DSW(39, 124, 3072) EX(88, 88) CV(55, 86) EX(89, 89) EX(90, 90)
          MF(144, 216, 52, R16(144))
          DSW(39, 128, 16384) EX(91, 91) CV(56, 88) DSW(39, 132, 17408) EX(92, 92) EX(93, 93) CV(57, 90)
          DSW(39, 136, 18432) EX(94, 94) EX(95, 95) DSW(39, 140, 19456) CV(58, 92) "ds_write_b32 v47, v47 offset:16\n\t" CV(59, 94)
          "v_mov_b32 v64, 0\n\tv_mov_b32 v65, 0\n\t"
          MF(144, 220, 56, R16(144))
          "7:\n\tds_read_b32 v45, v44 offset:16\n\t"
          DSR(160, 42, 19456) DSR(164, 41, 3072) DSR(168, 40, 3072) DSR(172, 42, 16384) DSR(176, 41, 0) DSR(180, 40, 0)
          DSR(184, 42, 17408) DSR(188, 41, 1024) DSR(192, 40, 1024) DSR(196, 42, 18432) DSR(200, 41, 2048) DSR(204, 40, 2048)
          "s_waitcnt lgkmcnt(12)\n\tv_cmp_eq_u32 vcc, 0, v45\n\ts_cbranch_vccnz 9b\n\t"
          "s_waitcnt lgkmcnt(9)\n\t" AD12(156, 160)
          "v_mov_b32 v60, v156\n\tv_mov_b32 v61, v156\n\ts_waitcnt lgkmcnt(6)\n\t" AD4(144, 172)
          "v_permlane32_swap_b32 v60, v61\n\t" AD4(144, 176) "v_rcp_f32 v62, v61\n\t" AD4(144, 180)
          "v_cndmask_b32 v63, 0, v62, s[72:73]\n\ts_waitcnt lgkmcnt(3)\n\t" AD12(148, 184)
          NRM(144, 62, 52) "s_waitcnt lgkmcnt(0)\n\t" AD12(152, 196) NRM(148, 62, 54) NRM(156, 63, 58) NRM(152, 62, 56)
          "v_maximum3_f32 v64, v64, v65, v65\n\tv_cmp_nle_f32 vcc, 0x17800000, v61\n\ts_mov_b32 s71, %18\n\t"
          "v_cmp_nle_f32 s[72:73], v64, s71\n\ts_or_b64 vcc, vcc, s[72:73]\n\ts_cbranch_scc1 8f\n\t"
          "buffer_store_dwordx4 v[52:55], v48, s[64:67], %19 offen sc1\n\t"
          "buffer_store_dwordx4 v[56:59], v48, s[64:67], %19 offen offset:1024 sc1\n"
          "8:\n\ts_cselect_b32 %3, 1, 0\n\ts_nop 0\n.Lcode_end:"
          : "+{s[64:67]}"(rsk), "+{s68}"(ko), "+{v45}"(pv), "=s"(bad)
          : "s"(qso + (w4 ^ 3) * 2048), "s"(qso + (w4 ^ 2) * 2048), "s"(qso + (w4 ^ 1) * 2048), "s"(qso + w4 * 2048),
            "v"(ep.h[0]), "v"(ep.h[1]), "v"(ep.h[2]), "v"(ep.h[3]), "v"(ep.ho),
            "{v48}"(vl), "{v47}"(lb + L_W + w4 * 4), "{v46}"(voffF), "{s70}"(tag), "i"(WS_END), "i"(BIG), "s"(oso),
            "{v44}"(lb + L_W + (l & 3) * 4), "{v43}"(lb + w4 * 4096 + vl)
          : "memory", "scc", "vcc", "s69", "s71", "s72", "s73", "s74", "s75", "s76", "s77", "s78", "s79", "s80", "s81", "v39", "v40", "v41", "v42", "v49",
            V10(5), V10(6), V10(7), V10(8), V10(9), V10(10), V10(11), V10(12), V10(13), V10(14), V10(15),
            V10(16), V10(17), V10(18), V10(19), V10(20), V10(21), V10(22), V10(23), V10(24), "v250", "v251", "v252", "v253", "v254", "v255");
    }
    if (bad) {
      const bf16x8 q0 = ld16(rsW, vl, qso + w4 * 2048), q1 = ld16(rsW, vl + 1024, qso + w4 * 2048);
      f32x16 zero;
#pragma unroll
      for (int i = 0; i < 16; i++) zero[i] = 0.f;
      const unsigned ks = WS_KV + head * 64 * 4096;
      float mx = -__builtin_inff();
      for (int kt = 0; kt < 64; kt++) {
        f32x16 s = MFMA32(ld16(rsW, l * 16, ks + kt * 4096), q0, zero);
        s = MFMA32(ld16(rsW, l * 16 + 1024, ks + kt * 4096), q1, s);
#pragma unroll
        for (int i = 0; i < 16; i++) mx = __builtin_fmaxf(mx, s[i]);
      }
      mx = __builtin_fmaxf(mx, other_half(mx));
      f32x16 o = zero;
      for (int kt = 0; kt < 64; kt++) {
        f32x16 s = MFMA32(ld16(rsW, l * 16, ks + kt * 4096), q0, zero);
        s = MFMA32(ld16(rsW, l * 16 + 1024, ks + kt * 4096), q1, s);
#pragma unroll
        for (int i = 0; i < 16; i++) s[i] = __builtin_amdgcn_exp2f(s[i] - mx);
        o = MFMA32(ld16(rsW, l * 16 + 2048, ks + kt * 4096), pack8(s, 0), o);
        o = MFMA32(ld16(rsW, l * 16 + 3072, ks + kt * 4096), pack8(s, 8), o);
      }
      float lsum = other_half(o[12]);
      lsum = l & 32 ? o[12] : lsum;
      const float r = __builtin_amdgcn_rcpf(lsum);
#pragma unroll
      for (int i = 0; i < 16; i++) o[i] = i >= 12 && (l & 32) ? 0.f : o[i] * r;
#pragma unroll
      for (int a = 0; a < 2; a++) {
        u32x4 v = __builtin_bit_cast(u32x4, pack8(o, 8 * a));
#pragma unroll
        for (int i = 0; i < 4; i++) v[i] = v[i] == ~0u ? ~1u : v[i];
        st16(rsW, v, l * 16 + a * 1024, oso);
      }
    }
  } else {
    if (cb >= 14) tag = phase1<true, 1>(x, wqkv, wd, wo, rsW, rsD, lds, rb, cb, r, fc, head, qb, w, ep);
    else tag = phase1<false, 1>(x, wqkv, wg, wu, rsW, rsD, lds, rb, cb, r, fc, head, qb, w, ep);
    const int l = lane();
    auto sth = [&](int m) {
      asm volatile("buffer_store_dwordx4 %0, %1, %2, %3 offen offset:%4 sc1\n\ts_nop 1"
                   :: "v"(ep.h[m]), "v"(ep.ho), "s"(rsW), "s"((m >> 1) * (112 * 1024)), "i"((m & 1) * 256) : "memory");
    };
#pragma unroll
    for (int m = 0; m < HS; m++) sth(m);
    SB();
    const unsigned f2 = opaque(l == 0 && w == 4 ? WS_F2 + (rb * 16 + cb) * 128 : OOB);
    if (idx < 28) {
    const unsigned voffR = voff_r(fc, l), voffD = opaque(voffR + w4 * (448 * 896)), ring = lb + w4 * 4096;
    const int i16 = l & 15;
    const int ab = (4 * (l >> 5) + (i16 >> 2)) * 128 + (((((l >> 4) & 1) * 2 + ((i16 & 3) >> 1)) ^ (((i16 >> 3) ^ (w4 & 1)) << 2)) << 4) + (i16 & 1) * 8;
    const int ad = ab + w4 * 4096 + (fr & 1) * 2048;
    ldsp aq[3][4];
#pragma unroll
    for (int i = 0; i < 12; i++) aq[i >> 2][i & 3] = opaque(lds + ((ad ^ ((i & 1) << 6) ^ ((i & 2) << 10)) + (i >> 2) * 65536));
    auto rdw = [&](int slot, int i) {
      const int o = (slot & 65535) + (i >> 1) * 1024;
      return cat(trread(aq[slot >> 16][i & 1] + o), trread(aq[slot >> 16][2 + (i & 1)] + o));
    };
    const int tt = fr * 2 + (w4 >> 1), nt = fc * 2 + (w4 & 1), ro = OROT;
    const unsigned xo = ((tt * 32 + (l & 31)) * D + nt * 32 + 8 * (l >> 5)) * 2;
    const unsigned vx = ring + l * 16, aw = lb + L_WO + ((w4 & 1) * 8 + ro * 2) * 3584 + ab;
    u32x16 va = {(unsigned)l * 16, vx, vx + 0x18000, vx ^ 0x1000, (vx ^ 0x2000) + 0x18000, lb + L_W + (w4 ^ 2) * 4, aw, aw ^ 64,
                 aw - (l >> 5) * 512, (aw ^ 64) - (l >> 5) * 512, lb + L_W + w4 * 4, lb + (l & 4 ? W_S : W_AX) + (l & 3) * 4, lb + L_W + (w4 ^ 1) * 4, xo, 0, 0};
    PIN(va);
    const unsigned voffO = opaque(voffR + w4 * (32 * 896)), voffO2 = opaque(voffR + w4 * (16 * 896));
    const unsigned vf = opaque((l & 8 ? WS_F3 + ((w4 & 1) * 8 + (l & 7)) * 128 : WS_F2 + (((7 * w4) >> 1) + (l & 3)) * 128) + (fr >> 1) * 2048);
    u32x4 id[2];
#pragma unroll
    for (int i = 0; i < 8; i++) id[i >> 2][i & 3] = (((l & 31) >> 1) - 4 * (l >> 5) == (i >> 2) * 8 + (i & 3)) ? 0x3f80u << (16 * (l & 1)) : 0u;
    PIN2(id[0], id[1]);
    SB();
#pragma unroll
    for (int m = HS; m < 4; m++) sth(m);
    if (P0 == 1) __builtin_amdgcn_s_setprio(0);
    lds_wait(lds + W_D, 8, l, 1);
    issue_w(rsD, voffD, 0, ring, PL[0]);
    WAITVM(4);
    lds_set(lds + W_H + w * 4);
    lds_wait(lds + W_H, 8, l, 1);
    __builtin_amdgcn_raw_buffer_store_b32(tag, rsW, f2, 0, 16);
    if (P0 == 2) __builtin_amdgcn_s_setprio(0);
    issue_w(rsD, voffD, 1, ring, PL[1]);
    const rsrc_t rsX = mkrsrc(x, S * D * 2), rsY = mkrsrc((void*)ka[6], S * D * 2), rsO = mkrsrc(wo, D * D * 2);
    const u32x4 xa = __builtin_amdgcn_raw_buffer_load_b128(rsX, va[13], 0, 0), xb = __builtin_amdgcn_raw_buffer_load_b128(rsX, va[13] + 32, 0, 0);
    issue_w(rsD, voffD, 2, ring, PL[2]);
#pragma unroll
    for (int t = 0; t < 4; t++)
#pragma unroll
      for (int i = 0; i < 16; i++) acc[t][i] = 0.f;
    acc[0] = MFMA32(__builtin_bit_cast(bf16x8, id[0]), __builtin_bit_cast(bf16x8, xa), acc[0]);
    acc[0] = MFMA32(__builtin_bit_cast(bf16x8, id[1]), __builtin_bit_cast(bf16x8, xb), acc[0]);
    PIN(acc[0]);
    poll(rsW, vf, tag);
    const unsigned hso = WS_H + (fr * 2 * 112 + w4 * 28) * 1024;
    bf16x8 hf[14][4], wr[14][4];
    auto ldh = [&](int p) {
#pragma unroll
      for (int i = 0; i < 4; i++) hf[p][i] = ld16(rsW, va[0] + (i >> 1) * 1024, hso + p * 2048 + ((i & 1) ^ (w4 >> 1)) * (112 * 1024));
    };
    auto take = [&](int p) {
#pragma unroll
      for (int i = 0; i < 4; i++) wr[p][i] = rdw(PL[p], i);
    };
#pragma unroll
    for (int p = 0; p < HD; p++) ldh(p);
    SB();
#pragma unroll
    for (int p = 0; p < 9; p++) {
      take(p);
      SB();
      __builtin_amdgcn_s_setprio(0);
      SB();
#pragma unroll
      for (int i = 0; i < 8; i++) acc[i & 3] = MFMA32(wr[p][(i >> 2) * 2 + (i & 1)], hf[p][(i >> 2) * 2 + ((i >> 1) & 1)], acc[i & 3]);
      SB();
      __builtin_amdgcn_s_setprio(BPRIO);
      SB();
      if (p == 5) {
        take(9); take(10);
        lds_set(lds + W_X + w4 * 4);
      }
      if (p < 2) issue_w(rsD, voffD, p + 9, ring, PL[p]);
      else if (p >= 6) issue_w(rsO, voffO, (p - 6) * 4, ring, PL[p]);
      else if (p >= 3) issue_w(rsD, voffD, p + 8, ring, PL[p]);
      if (p + HD < 9) ldh(p + HD);
#pragma unroll
      for (int q = 9; q < 14; q++) if (((XH >> (4 * (q - 9))) & 15) == p) ldh(q);
      SB();
    }
    gldsi(rsO, voffO2, 384 * 896, lb + w4 * 2048, L_WO + 49152);
    gldsi(rsO, voffO2, 392 * 896, lb + w4 * 2048, L_WO + 50176);
#pragma unroll
    for (int q = 9; q < 14; q++) if (((XH >> (4 * (q - 9))) & 15) > 8) ldh(q);
    WAITVM(14);
    take(11); take(12); take(13);
    auto cat4 = [](bf16x8 a, bf16x8 b, bf16x8 c, bf16x8 d) {
      const u32x4 e = __builtin_bit_cast(u32x4, a), f = __builtin_bit_cast(u32x4, b), g = __builtin_bit_cast(u32x4, c), h = __builtin_bit_cast(u32x4, d);
      return (u32x16){e[0], e[1], e[2], e[3], f[0], f[1], f[2], f[3], g[0], g[1], g[2], g[3], h[0], h[1], h[2], h[3]};
    };
    u32x16 tw[5], th[5];
#pragma unroll
    for (int p = 0; p < 5; p++) tw[p] = cat4(wr[9 + p][0], wr[9 + p][1], wr[9 + p][2], wr[9 + p][3]);
    th[0] = cat4(hf[9][0], hf[9][1], hf[9][2], hf[9][3]);
    th[1] = cat4(hf[10][0], hf[10][1], hf[10][2], hf[10][3]);
    th[2] = cat4(hf[11][1], hf[11][3], hf[12][1], hf[12][3]);
    th[3] = cat4(hf[11][0], hf[11][2], hf[12][0], hf[12][2]);
    th[4] = cat4(hf[13][0], hf[13][1], hf[13][2], hf[13][3]);
    __builtin_amdgcn_s_setprio(0);
#define LD1(r, s, o) "buffer_load_dwordx4 " R4(r) ", v224, %15, " s " offen offset:" #o " sc1\n\t"
#define LDO(r, s) LD1(r, s, 0) LD1(r+4, s, 1024) LD1(r+8, s, 2048) LD1(r+12, s, 3072)
#define NXT(s) "v_add_u32 v230, " s ", v230\n\tv_add_u32 v231, " s ", v231\n\tv_add_u32 v232, " s ", v232\n\tv_add_u32 v233, " s ", v233\n\t"
#define TR(r, a, o) "ds_read_b64_tr_b16 v[" #r ":" #r "+1], v" #a " offset:" #o "\n\t"
#define TRA(r, h) TR(r, 230, h*3584) TR(r+2, 230, h*3584+1024) TR(r+4, 231, h*3584) TR(r+6, 231, h*3584+1024)
#define TRB(r, h) TR(r+8, 230, h*3584+2048) TR(r+10, 232, h*3584+3072) TR(r+12, 231, h*3584+2048) TR(r+14, 233, h*3584+3072)
#define M3(d, a) "v_max3_u32 v" #d ", v[" #a "], v[" #a "+1], v" #d "\n\t"
#define MAXO(r)                                                                                             \
  "v_max_u32 v238, v[" #r "], v[" #r "+1]\n\tv_max_u32 v239, v[" #r "+2], v[" #r "+3]\n\t"                  \
  M3(238, r+4) M3(239, r+6) M3(238, r+8) M3(239, r+10) M3(238, r+12) M3(239, r+14)                          \
  "v_max_u32 v238, v238, v239\n\tv_cmp_eq_u32 vcc, -1, v238\n\t"
#define CHK(j, r, s)                                                                                        \
  "s_bitcmp1_b32 s80, " #j "\n\ts_cbranch_scc0 1f\n\t" MAXO(r) "s_bitset0_b32 s80, " #j "\n\ts_cbranch_vccz 1f\n\t" \
  "s_bitset1_b32 s80, " #j "\n\t" RSL LDO(r, s) "1:\n\t"
#define MA(d, a, b) MF(d, a, b, R16(d))
#define MU(r, a, b) MA(112, a+4, r) MA(96, a, r) MA(112, a+12, r+4) MA(96, a+8, r+4) MA(112, b+4, r+8) MA(96, b, r+8) MA(112, b+12, r+12) MA(96, b+8, r+12)
#define MM(j, r, a, b) "s_cmp_lg_u32 s81, " #j "\n\ts_cbranch_scc1 1f\n\ts_bitcmp1_b32 s80, " #j "\n\ts_cbranch_scc1 6f\n\ts_add_u32 s81, s81, 1\n\t" MU(r, a, b) "1:\n\t"
#define FAST(j, r, a, b, n) "s_waitcnt vmcnt(" #n ")\n\t" MAXO(r) "s_nop 0\n\ts_cbranch_vccnz 5f\n\ts_bitset0_b32 s80, " #j "\n\ts_add_u32 s81, s81, 1\n\t" MU(r, a, b)
#define STEP(o, p, w, n) "s_cmp_eq_u32 %18, " #n "\n\ts_cselect_b32 " w ", -3, 1\n\ts_lshl_b32 " o ", " w ", 12\n\ts_add_u32 " o ", " o ", " p "\n\ts_mul_i32 " w ", " w ", 7168\n\t"
#define TRH(r, h) TRA(r, h) TRB(r, h)
#define M2(d, w, h) MA(d, w, h) MA(d+16, w+4, h)
    asm volatile(
        STEP("s77", "%16", "s83", 3) STEP("s78", "s77", "s84", 2) STEP("s79", "s78", "s85", 1)
        "s_mov_b32 s80, 15\n\ts_mov_b32 s81, 0\n\ts_mov_b32 s82, 0x40000\n\t"
        "s_waitcnt vmcnt(0)\n\tds_write_b32 v234, v234 offset:112\n\t" TOUT
        "2:\n\ts_sleep 2\n\tds_read_b32 v238, v235\n\ts_waitcnt lgkmcnt(0)\n\tv_cmp_eq_u32 vcc, 0, v238\n\t"
        "s_cbranch_vccz 3f\n\ts_sub_u32 s82, s82, 1\n\ts_cbranch_scc0 2b\n\ts_mov_b32 s82, 0\n"
        "3:\n\t"
        M2(128, 16, 4) TRA(240, 0) M2(96, 16, 0) TRB(240, 0) M2(128, 24, 12) M2(96, 24, 8)
        M2(128, 48, 36) TRA(0, 1) M2(96, 48, 32) TRB(0, 1) M2(128, 56, 44) NXT("s83") TRA(16, 0) M2(96, 56, 40) TRB(16, 0)
        M2(128, 160, 64) TRA(32, 1) M2(128, 168, 68) TRB(32, 1) M2(128, 176, 72) NXT("s84") TRA(48, 0) M2(128, 184, 76) TRB(48, 0)
        M2(128, 208, 84) TRA(64, 1) M2(128, 216, 92) TRB(64, 1)
        M2(96, 160, 192) DSW(226, 128, 0) DSW(226, 132, 1024) MA(96, 168, 196) DSW(226, 136, 2048) MA(112, 172, 196) DSW(226, 140, 3072)
        MA(96, 176, 200) LDO(160, "%16") DSW(226, 144, 16384) MA(112, 180, 200) DSW(226, 148, 17408)
        MA(96, 184, 204) DSW(226, 152, 18432) MA(112, 188, 204) DSW(226, 156, 19456) "ds_write_b32 v234, v234 offset:128\n\t"
        MA(96, 208, 80) LDO(176, "s77") MA(112, 212, 80) MA(96, 216, 88) MA(112, 220, 88)
        "4:\n\tds_read_b32 v238, v229 offset:128\n\t"
        DSR(128, 228, 0) DSR(132, 228, 1024) DSR(136, 228, 2048) DSR(140, 228, 3072)
        DSR(144, 228, 16384) DSR(148, 228, 17408) DSR(152, 228, 18432) DSR(156, 228, 19456)
        "s_waitcnt lgkmcnt(8)\n\tv_cmp_eq_u32 vcc, 0, v238\n\ts_cbranch_vccz 7f\n\ts_sub_u32 s82, s82, 1\n\ts_cbranch_scc0 4b\n\ts_mov_b32 s82, 0\n"
        "7:\n\t" NXT("s85") TRH(80, 0)
        "s_waitcnt lgkmcnt(12)\n\t" AD4(96, 128) AD4(100, 132) AD4(104, 136) AD4(108, 140)
        "s_waitcnt lgkmcnt(8)\n\t" TRH(128, 1) AD4(112, 144) AD4(116, 148) AD4(120, 152) AD4(124, 156)
        LDO(192, "s78") "s_waitcnt vmcnt(8)\n\t" MAXO(160) "s_nop 0\n\ts_cbranch_vccz 1f\n\t" LDO(208, "s79") "s_branch 5f\n"
        "1:\n\ts_mov_b32 s80, 14\n\ts_mov_b32 s81, 1\n\t"
        MA(112, 244, 160) LD1(208, "s79", 0) MA(96, 240, 160) MA(112, 252, 164) LD1(212, "s79", 1024) MA(96, 248, 164)
        MA(112, 4, 168) LD1(216, "s79", 2048) MA(96, 0, 168) MA(112, 12, 172) LD1(220, "s79", 3072) MA(96, 8, 172)
        FAST(1, 176, 16, 32, 8) FAST(2, 192, 48, 64, 4)
        "5:\n\ts_waitcnt vmcnt(0)\n55:\n\t"
        CHK(0, 160, "%16") CHK(1, 176, "s77") CHK(2, 192, "s78") CHK(3, 208, "s79")
        MM(0, 160, 240, 0) MM(1, 176, 16, 32) MM(2, 192, 48, 64)
        "s_bitcmp1_b32 s80, 3\n\ts_cbranch_scc0 8f\n"
        "6:\n\ts_sub_u32 s82, s82, 1\n\ts_cbranch_scc0 5b\n\ts_mov_b32 s80, 0\n\ts_mov_b32 s82, 0\n\ts_branch 5b\n"
        "8:\n\ts_waitcnt lgkmcnt(0)\n\t"
        MA(112, 84, 208) MA(112, 92, 212) MA(112, 132, 216) MA(112, 140, 220) MA(96, 80, 208) MA(96, 88, 212)
        DSW(225, 112, 16384) DSW(225, 116, 17408) MA(96, 128, 216) DSW(225, 120, 18432) DSW(225, 124, 19456)
        "ds_write_b32 v234, v234 offset:144\n\t" MA(96, 136, 220)
        "9:\n\tds_read_b32 v238, v236 offset:144\n\t"
        DSR(0, 227, 16384) DSR(4, 227, 17408) DSR(8, 227, 18432) DSR(12, 227, 19456)
        "s_waitcnt lgkmcnt(0)\n\tv_cmp_eq_u32 vcc, 0, v238\n\ts_cbranch_vccz 1f\n\ts_sub_u32 s82, s82, 1\n\ts_cbranch_scc0 9b\n"
        "1:\n\ts_nop 3\n\t"
        AD4(96, 0) AD4(100, 4) CV(16, 96) CV(17, 98) CV(18, 100) CV(19, 102) AD4(104, 8)
        "v_permlane32_swap_b32 v16, v18\n\tv_permlane32_swap_b32 v17, v19\n\t" AD4(108, 12)
        "buffer_store_dwordx4 v[16:19], v237, %17, 0 offen" OM "\n\t"
        CV(20, 104) CV(21, 106) CV(22, 108) CV(23, 110) "s_nop 1\n\t"
        "v_permlane32_swap_b32 v20, v22\n\tv_permlane32_swap_b32 v21, v23\n\ts_nop 1\n\t"
        "buffer_store_dwordx4 v[20:23], v237, %17, 0 offen offset:32" OM "\n\ts_waitcnt vmcnt(2)\n\ts_nop 1"
        : "+{v[96:111]}"(acc[0]), "+{v[112:127]}"(acc[1]), "+{v[128:143]}"(acc[2]), "+{v[144:159]}"(acc[3]), "+{v[224:239]}"(va),
          "+{v[0:15]}"(th[0]), "+{v[16:31]}"(tw[0]), "+{v[32:47]}"(th[1]), "+{v[48:63]}"(tw[1]), "+{v[64:79]}"(th[2]), "+{v[80:95]}"(th[4]),
          "+{v[160:175]}"(tw[2]), "+{v[176:191]}"(tw[3]), "+{v[192:207]}"(th[3]), "+{v[208:223]}"(tw[4])
        : "s"(rsW), "s"(WS_O + (tt * 32 + (w4 & 1) * 16 + ro * 4) * 1024), "s"(rsY), "s"(ro)
        : "memory", "scc", "vcc", "s77", "s78", "s79", "s80", "s81", "s82", "s83", "s84", "s85",
          V10(24), "v250", "v251", "v252", "v253", "v254", "v255");
    } else {
#pragma unroll
    for (int m = HS; m < 4; m++) sth(m);
    __builtin_amdgcn_s_setprio(0);
    drain();
    lds_set(lds + W_H + w * 4);
    lds_set(lds + W_X + w4 * 4);
    lds_wait(lds + W_H, 8, l, 1);
    __builtin_amdgcn_raw_buffer_store_b32(tag, rsW, f2, 0, 16);
    const int gn = xcd * 4 + (idx & 3);
    touch(mkrsrc(wd, DF * D * 2), l < 35 ? (w4 * 448 + 288) * 896 + (gn * 35 + l) * 128 : OOB, lb + L_J);
    touch(mkrsrc(wo, D * D * 2), l < 25 ? ((gn * 4 + w4) * 25 + l) * 128 : OOB, lb + L_J);
    touch(mkrsrc((void*)ka[6], S * D * 2), w4 ? OOB : xcd * 229376 + l * 3584, lb + L_J);
    drain();
    if (b == 255 && w == 5) {
#pragma unroll
    for (int i = 0; i < 4; i++) poll(rsW, WS_F1 + (i * 64 + l) * 128, tag);
    __builtin_amdgcn_raw_buffer_store_b32(tag, rsW, l == 0 ? WS_EP : OOB, 0, 16);
    }
    }
  }
}

extern "C" void kernel_launch(void* const* d_in, const int* in_sizes, int n_in, void* d_out, int out_size, void* d_ws,
                              size_t ws_size, hipStream_t stream) {
  (void)hipFuncSetAttribute((const void*)fused, hipFuncAttributeMaxDynamicSharedMemorySize, LDS_BYTES);
  fused<<<256, 512, LDS_BYTES, stream>>>((const u16*)d_in[0], (const u16*)d_in[1], (const u16*)d_in[2], (const u16*)d_in[4],
                                         (const u16*)d_in[5], (const u16*)d_in[6], (u16*)d_out, (char*)d_ws);
}
```
